```python
import torch

def _softmax(x, axis=-1):
    return torch.softmax(x, dim=axis)
def _gelu(x):
    return torch.nn.functional.gelu(x, approximate="tanh")
def _silu(x):
    return torch.nn.functional.silu(x)
def _sigmoid(x):
    return torch.sigmoid(x)
def _layernorm(x, axis=-1, eps=1e-5):
    m = x.mean(dim=axis, keepdim=True)
    v = x.var(dim=axis, keepdim=True, unbiased=False)
    return (x - m) / torch.sqrt(v + eps)
def _scatter_add(tgt, idx, src, axis=0):
    return tgt.clone().index_add_(axis, idx, src)
def _segment_sum(src, seg, n):
    out = torch.zeros((n,) + src.shape[1:], dtype=src.dtype)
    return out.index_add_(0, seg, src)
def _pad(x, pw):
    flat = tuple(v for pair in reversed(pw) for v in pair)
    return torch.nn.functional.pad(x, flat)

kernel_name = 'concat_heads__sliding_block__spectral_fnet__prompt'

def setup_inputs():

    _w = (lambda a: (lambda t: t.to(torch.bfloat16) if t.is_floating_point() else t)(torch.as_tensor(a)))
    def _A_setup():
        B, S, D = 1, 2048, 1024
        return {
            "x": _w((lambda *s: torch.randn(*s, dtype=torch.bfloat16) * 0.2)(B, S, D)),
            "wqkv": _w((lambda *s: torch.randn(*s, dtype=torch.bfloat16) * 0.8)(D, 3 * D)),
            "wo": _w((lambda *s: torch.randn(*s, dtype=torch.bfloat16) * 0.2)(D, D)),
        }
    _A_in = _A_setup()

    def _B_setup():
        B, S, D = 1, 2048, 1024
        ns = torch.arange(S)[:, None] * torch.arange(S)[None, :]
        nd = torch.arange(D)[:, None] * torch.arange(D)[None, :]
        Ws_re = torch.cos(-2.0 * torch.pi * ns / S).to(float)
        Ws_im = torch.sin(-2.0 * torch.pi * ns / S).to(float)
        Wd_re = torch.cos(-2.0 * torch.pi * nd / D).to(float)
        Wd_im = torch.sin(-2.0 * torch.pi * nd / D).to(float)
        return {
            "x": _w((lambda *s: torch.randn(*s, dtype=torch.bfloat16) * 0.05)(B, S, D)),
            "dft_seq_re": _w(Ws_re),
            "dft_seq_im": _w(Ws_im),
            "dft_hid_re": _w(Wd_re),
            "dft_hid_im": _w(Wd_im),
        }
    _B_in = _B_setup()

    return {
        "A_x": _A_in["x"],
        "A_wqkv": _A_in["wqkv"],
        "A_wo": _A_in["wo"],
        "B_x": _B_in["x"],
        "B_dft_seq_re": _B_in["dft_seq_re"],
        "B_dft_seq_im": _B_in["dft_seq_im"],
        "B_dft_hid_re": _B_in["dft_hid_re"],
        "B_dft_hid_im": _B_in["dft_hid_im"],
    }

def reference(A_x, A_wqkv, A_wo, B_x, B_dft_seq_re, B_dft_seq_im, B_dft_hid_re, B_dft_hid_im):
    def _A_ref(x, wqkv, wo):
        B, S, D = x.shape
        H = 32; Dh = D // H; W = 256
        qkv = (lambda x, *d: x.permute(*d))((x @ wqkv).reshape(B, S, 3, H, Dh), 2, 0, 3, 1, 4)
        q, k, v = qkv[0], qkv[1], qkv[2]
        scores = torch.einsum("bhqd,bhkd->bhqk", q, k) * (Dh ** -0.5)
        idx = torch.arange(S)
        mask = (idx[:, None] - idx[None, :] >= 0) & (idx[:, None] - idx[None, :] < W)
        scores = torch.where(mask[None, None], scores, -1e9)
        p = _softmax(scores, axis=-1)
        o = torch.einsum("bhqk,bhkd->bhqd", p, v)
        return (lambda x, *d: x.permute(*d))(o, 0, 2, 1, 3).reshape(B, S, D) @ wo

    def _B_ref(x, dft_seq_re, dft_seq_im, dft_hid_re, dft_hid_im):
        a = torch.einsum("bsd,ed->bse", x, dft_hid_re)
        b = torch.einsum("bsd,ed->bse", x, dft_hid_im)
        y = torch.einsum("ts,bse->bte", dft_seq_re, a) - torch.einsum("ts,bse->bte", dft_seq_im, b)
        return y

    a = _A_ref(A_x, A_wqkv, A_wo)
    b = _B_ref(B_x, B_dft_seq_re, B_dft_seq_im, B_dft_hid_re, B_dft_hid_im)
    return torch.concatenate([a, b], axis=-1)
```

```cpp
#include <hip/hip_runtime.h>
#include <hip/hip_bf16.h>
#include <stdint.h>
#define MIMIC 0
typedef unsigned short u16;
__device__ __forceinline__ float bf2f(u16 v) { return __uint_as_float(((unsigned)v) << 16); }
__device__ __forceinline__ u16 f2bf(float f) { unsigned u = __float_as_uint(f); u += 0x7FFFu + ((u >> 16) & 1u); return (u16)(u >> 16); }
__device__ __forceinline__ float rnd(float f) { return bf2f(f2bf(f)); }

__global__ void gemm(const u16* A, long sam, long sak, const u16* B, long sbn, long sbk, float* Cf, u16* C, long ldc, int M, int N, int K) {
    __shared__ float sA[32][33], sB[32][33];
    int tx = threadIdx.x, ty = threadIdx.y;
    int m = blockIdx.y * 32 + ty, n = blockIdx.x * 32 + tx;
    float acc = 0.f;
    for (int k0 = 0; k0 < K; k0 += 32) {
        sA[ty][tx] = bf2f(A[(long)(blockIdx.y * 32 + ty) * sam + (long)(k0 + tx) * sak]);
        sB[ty][tx] = bf2f(B[(long)(blockIdx.x * 32 + ty) * sbn + (long)(k0 + tx) * sbk]);
        __syncthreads();
        for (int k = 0; k < 32; ++k) acc += sA[ty][k] * sB[tx][k];
        __syncthreads();
    }
    if (C) C[(long)m * ldc + n] = f2bf(acc);
    if (Cf) Cf[(long)m * ldc + n] = acc;
}
__global__ void attn(const u16* qkv, u16* o) {
    int r = blockIdx.x * 64 + threadIdx.x, h = blockIdx.y;
    float q[32];
    for (int d = 0; d < 32; ++d) q[d] = bf2f(qkv[(long)r * 3072 + h * 32 + d]);
    int k0 = r - 255 < 0 ? 0 : r - 255;
    float mx = -1e30f;
    for (int k = k0; k <= r; ++k) {
        float s = 0.f;
        for (int d = 0; d < 32; ++d) s += q[d] * bf2f(qkv[(long)k * 3072 + 1024 + h * 32 + d]);
        s = s * 0.17677669529663687f;
        mx = fmaxf(mx, s);
    }
    float l = 0.f;
    for (int k = k0; k <= r; ++k) {
        float s = 0.f;
        for (int d = 0; d < 32; ++d) s += q[d] * bf2f(qkv[(long)k * 3072 + 1024 + h * 32 + d]);
        s = s * 0.17677669529663687f;
        l += __expf(s - mx);
    }
    float acc[32];
    for (int d = 0; d < 32; ++d) acc[d] = 0.f;
    for (int k = k0; k <= r; ++k) {
        float s = 0.f;
        for (int d = 0; d < 32; ++d) s += q[d] * bf2f(qkv[(long)k * 3072 + 1024 + h * 32 + d]);
        s = s * 0.17677669529663687f;
        float p = rnd(__expf(s - mx));
        for (int d = 0; d < 32; ++d) acc[d] += p * bf2f(qkv[(long)k * 3072 + 2048 + h * 32 + d]);
    }
    for (int d = 0; d < 32; ++d) {
        o[(long)r * 1024 + h * 32 + d] = f2bf(acc[d] / l);
    }
}
__global__ void combine(const float* y1, const float* y2, u16* out) {
    int i = blockIdx.x * 256 + threadIdx.x;
    int t = i >> 10, e = i & 1023;
    out[(long)t * 2048 + 1024 + e] = f2bf(y1[i] - y2[i]);
}

extern "C" void kernel_launch(void* const* d_in, const int* in_sizes, int n_in, void* d_out, int out_size, void* d_ws, size_t ws_size, hipStream_t stream) {
    const u16* Ax = (const u16*)d_in[0]; const u16* wqkv = (const u16*)d_in[1]; const u16* wo = (const u16*)d_in[2];
    const u16* Bx = (const u16*)d_in[3]; const u16* Cr = (const u16*)d_in[4]; const u16* Ci = (const u16*)d_in[5];
    const u16* Dr = (const u16*)d_in[6]; const u16* Di = (const u16*)d_in[7];
    u16* out = (u16*)d_out;
    char* ws = (char*)d_ws;
    u16* qkv = (u16*)ws;
    u16* o = (u16*)(ws + (16 << 20));
    u16* a = (u16*)(ws + (24 << 20));
    u16* b = (u16*)(ws + (32 << 20));
    float* y1 = (float*)(ws + (40 << 20));
    float* y2 = (float*)(ws + (56 << 20));
    dim3 blk(32, 32);
    gemm<<<dim3(3072 / 32, 2048 / 32), blk, 0, stream>>>(Ax, 1024, 1, wqkv, 1, 3072, nullptr, qkv, 3072, 2048, 3072, 1024);
    attn<<<dim3(2048 / 64, 32), 64, 0, stream>>>(qkv, o);
    gemm<<<dim3(1024 / 32, 2048 / 32), blk, 0, stream>>>(o, 1024, 1, wo, 1, 1024, nullptr, out, 2048, 2048, 1024, 1024);
    gemm<<<dim3(1024 / 32, 2048 / 32), blk, 0, stream>>>(Bx, 1024, 1, Dr, 1024, 1, nullptr, a, 1024, 2048, 1024, 1024);
    gemm<<<dim3(1024 / 32, 2048 / 32), blk, 0, stream>>>(Bx, 1024, 1, Di, 1024, 1, nullptr, b, 1024, 2048, 1024, 1024);
    gemm<<<dim3(1024 / 32, 2048 / 32), blk, 0, stream>>>(Cr, 2048, 1, a, 1, 1024, y1, nullptr, 1024, 2048, 1024, 2048);
    gemm<<<dim3(1024 / 32, 2048 / 32), blk, 0, stream>>>(Ci, 2048, 1, b, 1, 1024, y2, nullptr, 1024, 2048, 1024, 2048);
    combine<<<2048 * 1024 / 256, 256, 0, stream>>>(y1, y2, out);
}
```

```cpp
#include <hip/hip_runtime.h>
#include <stdint.h>

#define GAS __attribute__((address_space(1)))
#define LAS __attribute__((address_space(3)))
typedef float f32x4 __attribute__((ext_vector_type(4)));
typedef float f32x32 __attribute__((ext_vector_type(32)));
typedef int i32x4 __attribute__((ext_vector_type(4)));
typedef int i32x8 __attribute__((ext_vector_type(8)));
typedef int i32x16 __attribute__((ext_vector_type(16)));
typedef unsigned u32x2 __attribute__((ext_vector_type(2)));
typedef unsigned u32x4 __attribute__((ext_vector_type(4)));
typedef __bf16 bf16x2 __attribute__((ext_vector_type(2)));
typedef __bf16 bf16x4 __attribute__((ext_vector_type(4)));
typedef __bf16 bf16x8 __attribute__((ext_vector_type(8)));
typedef unsigned long long u64;

#define ASM_T64_NT \
    "s_cmp_eq_u32 %[z], 0\n" \
    "s_cbranch_scc1 TOP%=\n" \
    "s_waitcnt lgkmcnt(0)\n" \
    "v_mfma_f32_16x16x32_bf16 v[0:3], v[80:83], v[64:67], 0\n" \
    "v_mfma_f32_16x16x32_bf16 v[4:7], v[84:87], v[64:67], 0\n" \
    "ds_read_b128 v[112:115], %[an1] offset:0\n" \
    "v_mfma_f32_16x16x32_bf16 v[8:11], v[88:91], v[64:67], 0\n" \
    "ds_read_b128 v[96:99], %[am1] offset:0\n" \
    "v_mfma_f32_16x16x32_bf16 v[12:15], v[92:95], v[64:67], 0\n" \
    "v_mfma_f32_16x16x32_bf16 v[16:19], v[80:83], v[68:71], 0\n" \
    "ds_read_b128 v[116:119], %[an1] offset:2048\n" \
    "v_mfma_f32_16x16x32_bf16 v[20:23], v[84:87], v[68:71], 0\n" \
    "ds_read_b128 v[100:103], %[am1] offset:2048\n" \
    "v_mfma_f32_16x16x32_bf16 v[24:27], v[88:91], v[68:71], 0\n" \
    "v_mfma_f32_16x16x32_bf16 v[28:31], v[92:95], v[68:71], 0\n" \
    "ds_read_b128 v[120:123], %[an1] offset:4096\n" \
    "v_mfma_f32_16x16x32_bf16 v[32:35], v[80:83], v[72:75], 0\n" \
    "ds_read_b128 v[104:107], %[am1] offset:4096\n" \
    "v_mfma_f32_16x16x32_bf16 v[36:39], v[84:87], v[72:75], 0\n" \
    "v_mfma_f32_16x16x32_bf16 v[40:43], v[88:91], v[72:75], 0\n" \
    "ds_read_b128 v[124:127], %[an1] offset:6144\n" \
    "v_mfma_f32_16x16x32_bf16 v[44:47], v[92:95], v[72:75], 0\n" \
    "ds_read_b128 v[108:111], %[am1] offset:6144\n" \
    "v_mfma_f32_16x16x32_bf16 v[48:51], v[80:83], v[76:79], 0\n" \
    "v_mfma_f32_16x16x32_bf16 v[52:55], v[84:87], v[76:79], 0\n" \
    "s_add_u32 %[slot], %[slot], 1\n" \
    "s_and_b32 %[slot], %[slot], 3\n" \
    "s_cmp_eq_u32 %[slot], 0\n" \
    "s_cselect_b32 %[dl], %[dwrap], %[dstep]\n" \
    "v_mfma_f32_16x16x32_bf16 v[56:59], v[88:91], v[76:79], 0\n" \
    "v_add_u32 %[am0], %[dl], %[am0]\n" \
    "v_add_u32 %[an0], %[dl], %[an0]\n" \
    "v_mfma_f32_16x16x32_bf16 v[60:63], v[92:95], v[76:79], 0\n" \
    "s_branch MID%=\n" \
    "TOP%=:\n" \
    "s_waitcnt lgkmcnt(0)\n" \
    "v_mfma_f32_16x16x32_bf16 v[0:3], v[80:83], v[64:67], v[0:3]\n" \
    "v_mfma_f32_16x16x32_bf16 v[4:7], v[84:87], v[64:67], v[4:7]\n" \
    "ds_read_b128 v[112:115], %[an1] offset:0\n" \
    "v_mfma_f32_16x16x32_bf16 v[8:11], v[88:91], v[64:67], v[8:11]\n" \
    "ds_read_b128 v[96:99], %[am1] offset:0\n" \
    "v_mfma_f32_16x16x32_bf16 v[12:15], v[92:95], v[64:67], v[12:15]\n" \
    "v_mfma_f32_16x16x32_bf16 v[16:19], v[80:83], v[68:71], v[16:19]\n" \
    "ds_read_b128 v[116:119], %[an1] offset:2048\n" \
    "v_mfma_f32_16x16x32_bf16 v[20:23], v[84:87], v[68:71], v[20:23]\n" \
    "ds_read_b128 v[100:103], %[am1] offset:2048\n" \
    "v_mfma_f32_16x16x32_bf16 v[24:27], v[88:91], v[68:71], v[24:27]\n" \
    "v_mfma_f32_16x16x32_bf16 v[28:31], v[92:95], v[68:71], v[28:31]\n" \
    "ds_read_b128 v[120:123], %[an1] offset:4096\n" \
    "v_mfma_f32_16x16x32_bf16 v[32:35], v[80:83], v[72:75], v[32:35]\n" \
    "ds_read_b128 v[104:107], %[am1] offset:4096\n" \
    "v_mfma_f32_16x16x32_bf16 v[36:39], v[84:87], v[72:75], v[36:39]\n" \
    "v_mfma_f32_16x16x32_bf16 v[40:43], v[88:91], v[72:75], v[40:43]\n" \
    "ds_read_b128 v[124:127], %[an1] offset:6144\n" \
    "v_mfma_f32_16x16x32_bf16 v[44:47], v[92:95], v[72:75], v[44:47]\n" \
    "ds_read_b128 v[108:111], %[am1] offset:6144\n" \
    "v_mfma_f32_16x16x32_bf16 v[48:51], v[80:83], v[76:79], v[48:51]\n" \
    "v_mfma_f32_16x16x32_bf16 v[52:55], v[84:87], v[76:79], v[52:55]\n" \
    "s_add_u32 %[slot], %[slot], 1\n" \
    "s_and_b32 %[slot], %[slot], 3\n" \
    "s_cmp_eq_u32 %[slot], 0\n" \
    "s_cselect_b32 %[dl], %[dwrap], %[dstep]\n" \
    "v_mfma_f32_16x16x32_bf16 v[56:59], v[88:91], v[76:79], v[56:59]\n" \
    "v_add_u32 %[am0], %[dl], %[am0]\n" \
    "v_add_u32 %[an0], %[dl], %[an0]\n" \
    "v_mfma_f32_16x16x32_bf16 v[60:63], v[92:95], v[76:79], v[60:63]\n" \
    "MID%=:\n" \
    "s_waitcnt lgkmcnt(0)\n" \
    "s_barrier\n" \
    "v_mfma_f32_16x16x32_bf16 v[0:3], v[112:115], v[96:99], v[0:3]\n" \
    "v_mfma_f32_16x16x32_bf16 v[4:7], v[116:119], v[96:99], v[4:7]\n" \
    "ds_read_b128 v[80:83], %[an0] offset:0\n" \
    "v_mfma_f32_16x16x32_bf16 v[8:11], v[120:123], v[96:99], v[8:11]\n" \
    "ds_read_b128 v[64:67], %[am0] offset:0\n" \
    "v_mfma_f32_16x16x32_bf16 v[12:15], v[124:127], v[96:99], v[12:15]\n" \
    "v_mfma_f32_16x16x32_bf16 v[16:19], v[112:115], v[100:103], v[16:19]\n" \
    "ds_read_b128 v[84:87], %[an0] offset:2048\n" \
    "v_mfma_f32_16x16x32_bf16 v[20:23], v[116:119], v[100:103], v[20:23]\n" \
    "ds_read_b128 v[68:71], %[am0] offset:2048\n" \
    "v_mfma_f32_16x16x32_bf16 v[24:27], v[120:123], v[100:103], v[24:27]\n" \
    "v_mfma_f32_16x16x32_bf16 v[28:31], v[124:127], v[100:103], v[28:31]\n" \
    "ds_read_b128 v[88:91], %[an0] offset:4096\n" \
    "v_mfma_f32_16x16x32_bf16 v[32:35], v[112:115], v[104:107], v[32:35]\n" \
    "ds_read_b128 v[72:75], %[am0] offset:4096\n" \
    "v_mfma_f32_16x16x32_bf16 v[36:39], v[116:119], v[104:107], v[36:39]\n" \
    "v_mfma_f32_16x16x32_bf16 v[40:43], v[120:123], v[104:107], v[40:43]\n" \
    "ds_read_b128 v[92:95], %[an0] offset:6144\n" \
    "v_mfma_f32_16x16x32_bf16 v[44:47], v[124:127], v[104:107], v[44:47]\n" \
    "ds_read_b128 v[76:79], %[am0] offset:6144\n" \
    "v_mfma_f32_16x16x32_bf16 v[48:51], v[112:115], v[108:111], v[48:51]\n" \
    "v_mfma_f32_16x16x32_bf16 v[52:55], v[116:119], v[108:111], v[52:55]\n" \
    "v_mfma_f32_16x16x32_bf16 v[56:59], v[120:123], v[108:111], v[56:59]\n" \
    "v_add_u32 %[am1], %[dl], %[am1]\n" \
    "v_add_u32 %[an1], %[dl], %[an1]\n" \
    "s_sub_u32 %[n], %[n], 1\n" \
    "v_mfma_f32_16x16x32_bf16 v[60:63], v[124:127], v[108:111], v[60:63]\n" \
    "s_cmp_lg_u32 %[n], 0\n" \
    "s_cbranch_scc1 TOP%=\n" \
    "s_waitcnt lgkmcnt(0)\n" \
    "s_nop 15\n" \
    ""

#define ASM_T64_TR \
    "s_cmp_eq_u32 %[z], 0\n" \
    "s_cbranch_scc1 TOP%=\n" \
    "s_waitcnt lgkmcnt(0)\n" \
    "v_mfma_f32_16x16x32_bf16 v[0:3], v[80:83], v[64:67], 0\n" \
    "v_mfma_f32_16x16x32_bf16 v[4:7], v[84:87], v[64:67], 0\n" \
    "ds_read_b64_tr_b16 v[112:113], %[an0] offset:8192\n" \
    "v_mfma_f32_16x16x32_bf16 v[8:11], v[88:91], v[64:67], 0\n" \
    "ds_read_b64_tr_b16 v[114:115], %[an0] offset:9216\n" \
    "v_mfma_f32_16x16x32_bf16 v[12:15], v[92:95], v[64:67], 0\n" \
    "ds_read_b128 v[96:99], %[am1] offset:0\n" \
    "v_mfma_f32_16x16x32_bf16 v[16:19], v[80:83], v[68:71], 0\n" \
    "ds_read_b64_tr_b16 v[116:117], %[an1] offset:8192\n" \
    "v_mfma_f32_16x16x32_bf16 v[20:23], v[84:87], v[68:71], 0\n" \
    "ds_read_b64_tr_b16 v[118:119], %[an1] offset:9216\n" \
    "v_mfma_f32_16x16x32_bf16 v[24:27], v[88:91], v[68:71], 0\n" \
    "ds_read_b128 v[100:103], %[am1] offset:2048\n" \
    "v_mfma_f32_16x16x32_bf16 v[28:31], v[92:95], v[68:71], 0\n" \
    "ds_read_b64_tr_b16 v[120:121], %[an2] offset:8192\n" \
    "v_mfma_f32_16x16x32_bf16 v[32:35], v[80:83], v[72:75], 0\n" \
    "ds_read_b64_tr_b16 v[122:123], %[an2] offset:9216\n" \
    "v_mfma_f32_16x16x32_bf16 v[36:39], v[84:87], v[72:75], 0\n" \
    "ds_read_b128 v[104:107], %[am1] offset:4096\n" \
    "v_mfma_f32_16x16x32_bf16 v[40:43], v[88:91], v[72:75], 0\n" \
    "ds_read_b64_tr_b16 v[124:125], %[an3] offset:8192\n" \
    "v_mfma_f32_16x16x32_bf16 v[44:47], v[92:95], v[72:75], 0\n" \
    "ds_read_b64_tr_b16 v[126:127], %[an3] offset:9216\n" \
    "v_mfma_f32_16x16x32_bf16 v[48:51], v[80:83], v[76:79], 0\n" \
    "ds_read_b128 v[108:111], %[am1] offset:6144\n" \
    "v_mfma_f32_16x16x32_bf16 v[52:55], v[84:87], v[76:79], 0\n" \
    "s_add_u32 %[slot], %[slot], 1\n" \
    "s_and_b32 %[slot], %[slot], 3\n" \
    "s_cmp_eq_u32 %[slot], 0\n" \
    "s_cselect_b32 %[dl], %[dwrap], %[dstep]\n" \
    "v_mfma_f32_16x16x32_bf16 v[56:59], v[88:91], v[76:79], 0\n" \
    "v_add_u32 %[am0], %[dl], %[am0]\n" \
    "v_mfma_f32_16x16x32_bf16 v[60:63], v[92:95], v[76:79], 0\n" \
    "v_add_u32 %[an0], %[dl], %[an0]\n" \
    "v_add_u32 %[an1], %[dl], %[an1]\n" \
    "v_add_u32 %[an2], %[dl], %[an2]\n" \
    "v_add_u32 %[an3], %[dl], %[an3]\n" \
    "s_branch MID%=\n" \
    "TOP%=:\n" \
    "s_waitcnt lgkmcnt(0)\n" \
    "v_mfma_f32_16x16x32_bf16 v[0:3], v[80:83], v[64:67], v[0:3]\n" \
    "v_mfma_f32_16x16x32_bf16 v[4:7], v[84:87], v[64:67], v[4:7]\n" \
    "ds_read_b64_tr_b16 v[112:113], %[an0] offset:8192\n" \
    "v_mfma_f32_16x16x32_bf16 v[8:11], v[88:91], v[64:67], v[8:11]\n" \
    "ds_read_b64_tr_b16 v[114:115], %[an0] offset:9216\n" \
    "v_mfma_f32_16x16x32_bf16 v[12:15], v[92:95], v[64:67], v[12:15]\n" \
    "ds_read_b128 v[96:99], %[am1] offset:0\n" \
    "v_mfma_f32_16x16x32_bf16 v[16:19], v[80:83], v[68:71], v[16:19]\n" \
    "ds_read_b64_tr_b16 v[116:117], %[an1] offset:8192\n" \
    "v_mfma_f32_16x16x32_bf16 v[20:23], v[84:87], v[68:71], v[20:23]\n" \
    "ds_read_b64_tr_b16 v[118:119], %[an1] offset:9216\n" \
    "v_mfma_f32_16x16x32_bf16 v[24:27], v[88:91], v[68:71], v[24:27]\n" \
    "ds_read_b128 v[100:103], %[am1] offset:2048\n" \
    "v_mfma_f32_16x16x32_bf16 v[28:31], v[92:95], v[68:71], v[28:31]\n" \
    "ds_read_b64_tr_b16 v[120:121], %[an2] offset:8192\n" \
    "v_mfma_f32_16x16x32_bf16 v[32:35], v[80:83], v[72:75], v[32:35]\n" \
    "ds_read_b64_tr_b16 v[122:123], %[an2] offset:9216\n" \
    "v_mfma_f32_16x16x32_bf16 v[36:39], v[84:87], v[72:75], v[36:39]\n" \
    "ds_read_b128 v[104:107], %[am1] offset:4096\n" \
    "v_mfma_f32_16x16x32_bf16 v[40:43], v[88:91], v[72:75], v[40:43]\n" \
    "ds_read_b64_tr_b16 v[124:125], %[an3] offset:8192\n" \
    "v_mfma_f32_16x16x32_bf16 v[44:47], v[92:95], v[72:75], v[44:47]\n" \
    "ds_read_b64_tr_b16 v[126:127], %[an3] offset:9216\n" \
    "v_mfma_f32_16x16x32_bf16 v[48:51], v[80:83], v[76:79], v[48:51]\n" \
    "ds_read_b128 v[108:111], %[am1] offset:6144\n" \
    "v_mfma_f32_16x16x32_bf16 v[52:55], v[84:87], v[76:79], v[52:55]\n" \
    "s_add_u32 %[slot], %[slot], 1\n" \
    "s_and_b32 %[slot], %[slot], 3\n" \
    "s_cmp_eq_u32 %[slot], 0\n" \
    "s_cselect_b32 %[dl], %[dwrap], %[dstep]\n" \
    "v_mfma_f32_16x16x32_bf16 v[56:59], v[88:91], v[76:79], v[56:59]\n" \
    "v_add_u32 %[am0], %[dl], %[am0]\n" \
    "v_mfma_f32_16x16x32_bf16 v[60:63], v[92:95], v[76:79], v[60:63]\n" \
    "v_add_u32 %[an0], %[dl], %[an0]\n" \
    "v_add_u32 %[an1], %[dl], %[an1]\n" \
    "v_add_u32 %[an2], %[dl], %[an2]\n" \
    "v_add_u32 %[an3], %[dl], %[an3]\n" \
    "MID%=:\n" \
    "s_waitcnt lgkmcnt(0)\n" \
    "s_barrier\n" \
    "v_mfma_f32_16x16x32_bf16 v[0:3], v[112:115], v[96:99], v[0:3]\n" \
    "v_mfma_f32_16x16x32_bf16 v[4:7], v[116:119], v[96:99], v[4:7]\n" \
    "ds_read_b64_tr_b16 v[80:81], %[an0] offset:0\n" \
    "v_mfma_f32_16x16x32_bf16 v[8:11], v[120:123], v[96:99], v[8:11]\n" \
    "ds_read_b64_tr_b16 v[82:83], %[an0] offset:1024\n" \
    "v_mfma_f32_16x16x32_bf16 v[12:15], v[124:127], v[96:99], v[12:15]\n" \
    "ds_read_b128 v[64:67], %[am0] offset:0\n" \
    "v_mfma_f32_16x16x32_bf16 v[16:19], v[112:115], v[100:103], v[16:19]\n" \
    "ds_read_b64_tr_b16 v[84:85], %[an1] offset:0\n" \
    "v_mfma_f32_16x16x32_bf16 v[20:23], v[116:119], v[100:103], v[20:23]\n" \
    "ds_read_b64_tr_b16 v[86:87], %[an1] offset:1024\n" \
    "v_mfma_f32_16x16x32_bf16 v[24:27], v[120:123], v[100:103], v[24:27]\n" \
    "ds_read_b128 v[68:71], %[am0] offset:2048\n" \
    "v_mfma_f32_16x16x32_bf16 v[28:31], v[124:127], v[100:103], v[28:31]\n" \
    "ds_read_b64_tr_b16 v[88:89], %[an2] offset:0\n" \
    "v_mfma_f32_16x16x32_bf16 v[32:35], v[112:115], v[104:107], v[32:35]\n" \
    "ds_read_b64_tr_b16 v[90:91], %[an2] offset:1024\n" \
    "v_mfma_f32_16x16x32_bf16 v[36:39], v[116:119], v[104:107], v[36:39]\n" \
    "ds_read_b128 v[72:75], %[am0] offset:4096\n" \
    "v_mfma_f32_16x16x32_bf16 v[40:43], v[120:123], v[104:107], v[40:43]\n" \
    "ds_read_b64_tr_b16 v[92:93], %[an3] offset:0\n" \
    "v_mfma_f32_16x16x32_bf16 v[44:47], v[124:127], v[104:107], v[44:47]\n" \
    "ds_read_b64_tr_b16 v[94:95], %[an3] offset:1024\n" \
    "v_mfma_f32_16x16x32_bf16 v[48:51], v[112:115], v[108:111], v[48:51]\n" \
    "ds_read_b128 v[76:79], %[am0] offset:6144\n" \
    "v_mfma_f32_16x16x32_bf16 v[52:55], v[116:119], v[108:111], v[52:55]\n" \
    "v_mfma_f32_16x16x32_bf16 v[56:59], v[120:123], v[108:111], v[56:59]\n" \
    "v_add_u32 %[am1], %[dl], %[am1]\n" \
    "s_sub_u32 %[n], %[n], 1\n" \
    "v_mfma_f32_16x16x32_bf16 v[60:63], v[124:127], v[108:111], v[60:63]\n" \
    "s_cmp_lg_u32 %[n], 0\n" \
    "s_cbranch_scc1 TOP%=\n" \
    "s_waitcnt lgkmcnt(0)\n" \
    "s_nop 15\n" \
    ""

#define ASM_QKV \
    "ds_read_b64_tr_b16 v[128:129], %[an0] offset:0\n" \
    "ds_read_b64_tr_b16 v[130:131], %[an0] offset:512\n" \
    "ds_read_b128 v[96:99], %[am0] offset:0\n" \
    "ds_read_b64_tr_b16 v[132:133], %[an1] offset:0\n" \
    "ds_read_b64_tr_b16 v[134:135], %[an1] offset:512\n" \
    "ds_read_b128 v[100:103], %[am0] offset:2048\n" \
    "ds_read_b64_tr_b16 v[136:137], %[an0] offset:8192\n" \
    "ds_read_b64_tr_b16 v[138:139], %[an0] offset:8704\n" \
    "ds_read_b128 v[104:107], %[am0] offset:4096\n" \
    "ds_read_b64_tr_b16 v[140:141], %[an1] offset:8192\n" \
    "ds_read_b64_tr_b16 v[142:143], %[an1] offset:8704\n" \
    "ds_read_b128 v[108:111], %[am0] offset:6144\n" \
    "ds_read_b64_tr_b16 v[144:145], %[an0] offset:16384\n" \
    "ds_read_b64_tr_b16 v[146:147], %[an0] offset:16896\n" \
    "ds_read_b64_tr_b16 v[148:149], %[an1] offset:16384\n" \
    "ds_read_b64_tr_b16 v[150:151], %[an1] offset:16896\n" \
    "s_waitcnt lgkmcnt(4)\n" \
    "v_mfma_f32_16x16x32_bf16 v[0:3], v[128:131], v[96:99], 0\n" \
    "v_mfma_f32_16x16x32_bf16 v[24:27], v[128:131], v[100:103], 0\n" \
    "v_mfma_f32_16x16x32_bf16 v[48:51], v[128:131], v[104:107], 0\n" \
    "v_mfma_f32_16x16x32_bf16 v[72:75], v[128:131], v[108:111], 0\n" \
    "ds_read_b64_tr_b16 v[128:129], %[an0] offset:4096\n" \
    "ds_read_b64_tr_b16 v[130:131], %[an0] offset:4608\n" \
    "ds_read_b128 v[112:115], %[am1] offset:0\n" \
    "v_mfma_f32_16x16x32_bf16 v[4:7], v[132:135], v[96:99], 0\n" \
    "v_mfma_f32_16x16x32_bf16 v[28:31], v[132:135], v[100:103], 0\n" \
    "v_mfma_f32_16x16x32_bf16 v[52:55], v[132:135], v[104:107], 0\n" \
    "v_mfma_f32_16x16x32_bf16 v[76:79], v[132:135], v[108:111], 0\n" \
    "ds_read_b64_tr_b16 v[132:133], %[an1] offset:4096\n" \
    "ds_read_b64_tr_b16 v[134:135], %[an1] offset:4608\n" \
    "ds_read_b128 v[116:119], %[am1] offset:2048\n" \
    "v_mfma_f32_16x16x32_bf16 v[8:11], v[136:139], v[96:99], 0\n" \
    "v_mfma_f32_16x16x32_bf16 v[32:35], v[136:139], v[100:103], 0\n" \
    "v_mfma_f32_16x16x32_bf16 v[56:59], v[136:139], v[104:107], 0\n" \
    "v_mfma_f32_16x16x32_bf16 v[80:83], v[136:139], v[108:111], 0\n" \
    "ds_read_b64_tr_b16 v[136:137], %[an0] offset:12288\n" \
    "ds_read_b64_tr_b16 v[138:139], %[an0] offset:12800\n" \
    "ds_read_b128 v[120:123], %[am1] offset:4096\n" \
    "v_mfma_f32_16x16x32_bf16 v[12:15], v[140:143], v[96:99], 0\n" \
    "v_mfma_f32_16x16x32_bf16 v[36:39], v[140:143], v[100:103], 0\n" \
    "v_mfma_f32_16x16x32_bf16 v[60:63], v[140:143], v[104:107], 0\n" \
    "v_mfma_f32_16x16x32_bf16 v[84:87], v[140:143], v[108:111], 0\n" \
    "ds_read_b64_tr_b16 v[140:141], %[an1] offset:12288\n" \
    "ds_read_b64_tr_b16 v[142:143], %[an1] offset:12800\n" \
    "ds_read_b128 v[124:127], %[am1] offset:6144\n" \
    "s_waitcnt lgkmcnt(14)\n" \
    "v_mfma_f32_16x16x32_bf16 v[16:19], v[144:147], v[96:99], 0\n" \
    "v_mfma_f32_16x16x32_bf16 v[40:43], v[144:147], v[100:103], 0\n" \
    "v_mfma_f32_16x16x32_bf16 v[64:67], v[144:147], v[104:107], 0\n" \
    "v_mfma_f32_16x16x32_bf16 v[88:91], v[144:147], v[108:111], 0\n" \
    "ds_read_b64_tr_b16 v[144:145], %[an0] offset:20480\n" \
    "ds_read_b64_tr_b16 v[146:147], %[an0] offset:20992\n" \
    "s_add_u32 %[ms], %[ms], 1\n" \
    "s_cmp_eq_u32 %[ms], 3\n" \
    "s_cselect_b32 %[ms], 0, %[ms]\n" \
    "s_cmp_eq_u32 %[ms], 0\n" \
    "s_cselect_b32 %[dm], %[mwrap], %[mstep]\n" \
    "s_xor_b32 %[ns], %[ns], 1\n" \
    "s_cmp_eq_u32 %[ns], 0\n" \
    "s_cselect_b32 %[dn], %[nwrap], %[nstep]\n" \
    "s_waitcnt lgkmcnt(14)\n" \
    "v_mfma_f32_16x16x32_bf16 v[20:23], v[148:151], v[96:99], 0\n" \
    "v_mfma_f32_16x16x32_bf16 v[44:47], v[148:151], v[100:103], 0\n" \
    "v_mfma_f32_16x16x32_bf16 v[68:71], v[148:151], v[104:107], 0\n" \
    "v_mfma_f32_16x16x32_bf16 v[92:95], v[148:151], v[108:111], 0\n" \
    "ds_read_b64_tr_b16 v[148:149], %[an1] offset:20480\n" \
    "ds_read_b64_tr_b16 v[150:151], %[an1] offset:20992\n" \
    "v_add_u32 %[am0], %[dm], %[am0]\n" \
    "v_add_u32 %[am1], %[dm], %[am1]\n" \
    "v_add_u32 %[an0], %[dn], %[an0]\n" \
    "v_add_u32 %[an1], %[dn], %[an1]\n" \
    "s_branch MID%=\n" \
    "TOP%=:\n" \
    "s_waitcnt lgkmcnt(4)\n" \
    "v_mfma_f32_16x16x32_bf16 v[0:3], v[128:131], v[96:99], v[0:3]\n" \
    "v_mfma_f32_16x16x32_bf16 v[24:27], v[128:131], v[100:103], v[24:27]\n" \
    "v_mfma_f32_16x16x32_bf16 v[48:51], v[128:131], v[104:107], v[48:51]\n" \
    "v_mfma_f32_16x16x32_bf16 v[72:75], v[128:131], v[108:111], v[72:75]\n" \
    "ds_read_b64_tr_b16 v[128:129], %[an0] offset:4096\n" \
    "ds_read_b64_tr_b16 v[130:131], %[an0] offset:4608\n" \
    "ds_read_b128 v[112:115], %[am1] offset:0\n" \
    "v_mfma_f32_16x16x32_bf16 v[4:7], v[132:135], v[96:99], v[4:7]\n" \
    "v_mfma_f32_16x16x32_bf16 v[28:31], v[132:135], v[100:103], v[28:31]\n" \
    "v_mfma_f32_16x16x32_bf16 v[52:55], v[132:135], v[104:107], v[52:55]\n" \
    "v_mfma_f32_16x16x32_bf16 v[76:79], v[132:135], v[108:111], v[76:79]\n" \
    "ds_read_b64_tr_b16 v[132:133], %[an1] offset:4096\n" \
    "ds_read_b64_tr_b16 v[134:135], %[an1] offset:4608\n" \
    "ds_read_b128 v[116:119], %[am1] offset:2048\n" \
    "v_mfma_f32_16x16x32_bf16 v[8:11], v[136:139], v[96:99], v[8:11]\n" \
    "v_mfma_f32_16x16x32_bf16 v[32:35], v[136:139], v[100:103], v[32:35]\n" \
    "v_mfma_f32_16x16x32_bf16 v[56:59], v[136:139], v[104:107], v[56:59]\n" \
    "v_mfma_f32_16x16x32_bf16 v[80:83], v[136:139], v[108:111], v[80:83]\n" \
    "ds_read_b64_tr_b16 v[136:137], %[an0] offset:12288\n" \
    "ds_read_b64_tr_b16 v[138:139], %[an0] offset:12800\n" \
    "ds_read_b128 v[120:123], %[am1] offset:4096\n" \
    "v_mfma_f32_16x16x32_bf16 v[12:15], v[140:143], v[96:99], v[12:15]\n" \
    "v_mfma_f32_16x16x32_bf16 v[36:39], v[140:143], v[100:103], v[36:39]\n" \
    "v_mfma_f32_16x16x32_bf16 v[60:63], v[140:143], v[104:107], v[60:63]\n" \
    "v_mfma_f32_16x16x32_bf16 v[84:87], v[140:143], v[108:111], v[84:87]\n" \
    "ds_read_b64_tr_b16 v[140:141], %[an1] offset:12288\n" \
    "ds_read_b64_tr_b16 v[142:143], %[an1] offset:12800\n" \
    "ds_read_b128 v[124:127], %[am1] offset:6144\n" \
    "s_waitcnt lgkmcnt(14)\n" \
    "v_mfma_f32_16x16x32_bf16 v[16:19], v[144:147], v[96:99], v[16:19]\n" \
    "v_mfma_f32_16x16x32_bf16 v[40:43], v[144:147], v[100:103], v[40:43]\n" \
    "v_mfma_f32_16x16x32_bf16 v[64:67], v[144:147], v[104:107], v[64:67]\n" \
    "v_mfma_f32_16x16x32_bf16 v[88:91], v[144:147], v[108:111], v[88:91]\n" \
    "ds_read_b64_tr_b16 v[144:145], %[an0] offset:20480\n" \
    "ds_read_b64_tr_b16 v[146:147], %[an0] offset:20992\n" \
    "s_add_u32 %[ms], %[ms], 1\n" \
    "s_cmp_eq_u32 %[ms], 3\n" \
    "s_cselect_b32 %[ms], 0, %[ms]\n" \
    "s_cmp_eq_u32 %[ms], 0\n" \
    "s_cselect_b32 %[dm], %[mwrap], %[mstep]\n" \
    "s_xor_b32 %[ns], %[ns], 1\n" \
    "s_cmp_eq_u32 %[ns], 0\n" \
    "s_cselect_b32 %[dn], %[nwrap], %[nstep]\n" \
    "s_waitcnt lgkmcnt(14)\n" \
    "v_mfma_f32_16x16x32_bf16 v[20:23], v[148:151], v[96:99], v[20:23]\n" \
    "v_mfma_f32_16x16x32_bf16 v[44:47], v[148:151], v[100:103], v[44:47]\n" \
    "v_mfma_f32_16x16x32_bf16 v[68:71], v[148:151], v[104:107], v[68:71]\n" \
    "v_mfma_f32_16x16x32_bf16 v[92:95], v[148:151], v[108:111], v[92:95]\n" \
    "ds_read_b64_tr_b16 v[148:149], %[an1] offset:20480\n" \
    "ds_read_b64_tr_b16 v[150:151], %[an1] offset:20992\n" \
    "v_add_u32 %[am0], %[dm], %[am0]\n" \
    "v_add_u32 %[am1], %[dm], %[am1]\n" \
    "v_add_u32 %[an0], %[dn], %[an0]\n" \
    "v_add_u32 %[an1], %[dn], %[an1]\n" \
    "MID%=:\n" \
    "s_waitcnt lgkmcnt(4)\n" \
    "v_mfma_f32_16x16x32_bf16 v[0:3], v[128:131], v[112:115], v[0:3]\n" \
    "v_mfma_f32_16x16x32_bf16 v[24:27], v[128:131], v[116:119], v[24:27]\n" \
    "v_mfma_f32_16x16x32_bf16 v[48:51], v[128:131], v[120:123], v[48:51]\n" \
    "v_mfma_f32_16x16x32_bf16 v[72:75], v[128:131], v[124:127], v[72:75]\n" \
    "v_mfma_f32_16x16x32_bf16 v[4:7], v[132:135], v[112:115], v[4:7]\n" \
    "v_mfma_f32_16x16x32_bf16 v[28:31], v[132:135], v[116:119], v[28:31]\n" \
    "v_mfma_f32_16x16x32_bf16 v[52:55], v[132:135], v[120:123], v[52:55]\n" \
    "v_mfma_f32_16x16x32_bf16 v[76:79], v[132:135], v[124:127], v[76:79]\n" \
    "s_waitcnt lgkmcnt(0)\n" \
    "s_barrier\n" \
    "ds_read_b64_tr_b16 v[128:129], %[an0] offset:0\n" \
    "ds_read_b64_tr_b16 v[130:131], %[an0] offset:512\n" \
    "ds_read_b64_tr_b16 v[132:133], %[an1] offset:0\n" \
    "ds_read_b64_tr_b16 v[134:135], %[an1] offset:512\n" \
    "v_mfma_f32_16x16x32_bf16 v[8:11], v[136:139], v[112:115], v[8:11]\n" \
    "v_mfma_f32_16x16x32_bf16 v[32:35], v[136:139], v[116:119], v[32:35]\n" \
    "v_mfma_f32_16x16x32_bf16 v[56:59], v[136:139], v[120:123], v[56:59]\n" \
    "v_mfma_f32_16x16x32_bf16 v[80:83], v[136:139], v[124:127], v[80:83]\n" \
    "ds_read_b64_tr_b16 v[136:137], %[an0] offset:8192\n" \
    "ds_read_b64_tr_b16 v[138:139], %[an0] offset:8704\n" \
    "ds_read_b128 v[96:99], %[am0] offset:0\n" \
    "ds_read_b128 v[100:103], %[am0] offset:2048\n" \
    "v_mfma_f32_16x16x32_bf16 v[12:15], v[140:143], v[112:115], v[12:15]\n" \
    "v_mfma_f32_16x16x32_bf16 v[36:39], v[140:143], v[116:119], v[36:39]\n" \
    "v_mfma_f32_16x16x32_bf16 v[60:63], v[140:143], v[120:123], v[60:63]\n" \
    "v_mfma_f32_16x16x32_bf16 v[84:87], v[140:143], v[124:127], v[84:87]\n" \
    "ds_read_b64_tr_b16 v[140:141], %[an1] offset:8192\n" \
    "ds_read_b64_tr_b16 v[142:143], %[an1] offset:8704\n" \
    "ds_read_b128 v[104:107], %[am0] offset:4096\n" \
    "ds_read_b128 v[108:111], %[am0] offset:6144\n" \
    "v_mfma_f32_16x16x32_bf16 v[16:19], v[144:147], v[112:115], v[16:19]\n" \
    "v_mfma_f32_16x16x32_bf16 v[40:43], v[144:147], v[116:119], v[40:43]\n" \
    "v_mfma_f32_16x16x32_bf16 v[64:67], v[144:147], v[120:123], v[64:67]\n" \
    "v_mfma_f32_16x16x32_bf16 v[88:91], v[144:147], v[124:127], v[88:91]\n" \
    "ds_read_b64_tr_b16 v[144:145], %[an0] offset:16384\n" \
    "ds_read_b64_tr_b16 v[146:147], %[an0] offset:16896\n" \
    "s_sub_u32 %[n], %[n], 1\n" \
    "v_mfma_f32_16x16x32_bf16 v[20:23], v[148:151], v[112:115], v[20:23]\n" \
    "v_mfma_f32_16x16x32_bf16 v[44:47], v[148:151], v[116:119], v[44:47]\n" \
    "v_mfma_f32_16x16x32_bf16 v[68:71], v[148:151], v[120:123], v[68:71]\n" \
    "v_mfma_f32_16x16x32_bf16 v[92:95], v[148:151], v[124:127], v[92:95]\n" \
    "ds_read_b64_tr_b16 v[148:149], %[an1] offset:16384\n" \
    "ds_read_b64_tr_b16 v[150:151], %[an1] offset:16896\n" \
    "s_cmp_lg_u32 %[n], 0\n" \
    "s_cbranch_scc1 TOP%=\n" \
    "s_waitcnt lgkmcnt(0)\n" \
    "s_nop 15\n" \
    ""

static constexpr size_t WS_ABT = 0;
static constexpr size_t WS_O = 8u << 20;
static constexpr size_t WS_KV = 12u << 20;
static constexpr size_t WS_CTL = 24u << 20;
static constexpr int CTL_EPOCH = 0;
static constexpr int CTL_ARR = 1;
static constexpr int CTL_FA = CTL_ARR + 256;
static constexpr int CTL_FB = CTL_FA + 128;
static constexpr int CTL_KV = CTL_FB + 128;
static constexpr int CTL_FO = CTL_KV + 128;

static constexpr int LDS_BYTES = 160 * 1024;
static constexpr int LDS_MAIL = 128 * 1024;
#define DRAIN_STEPS 6
#define PF_DIST 0
static constexpr int SPIN_LIMIT = 1 << 17;

__device__ __forceinline__ void wg_barrier() { asm volatile("s_barrier" ::: "memory"); }
__device__ __forceinline__ int uni(int v) { return __builtin_amdgcn_readfirstlane(v); }
__device__ __forceinline__ int fresh(int v) { asm volatile("" : "+v"(v)); return v; }

__device__ __forceinline__ i32x4 make_srd(const void* p, unsigned bytes) {
    i32x4 r;
    r[0] = uni((int)(unsigned)(size_t)p);
    r[1] = uni((int)(unsigned)((size_t)p >> 32));
    r[2] = (int)bytes;
    r[3] = 0x00020000;
    return r;
}
__device__ __forceinline__ void dma16(unsigned voff, i32x4 srd, unsigned soff, unsigned lds) {
    asm volatile("s_mov_b32 m0, %3\n\ts_nop 0\n\tbuffer_load_dwordx4 %0, %1, %2 offen lds"
                 :: "v"(voff), "s"(srd), "s"(soff), "s"(lds) : "memory");
}
__device__ __forceinline__ void dma16_dev(unsigned voff, i32x4 srd, unsigned soff, unsigned lds) {
    asm volatile("s_mov_b32 m0, %3\n\ts_nop 0\n\tbuffer_load_dwordx4 %0, %1, %2 offen sc1 lds"
                 :: "v"(voff), "s"(srd), "s"(soff), "s"(lds) : "memory");
}
template <int N> __device__ __forceinline__ void wait_vm() { asm volatile("s_waitcnt vmcnt(%0)" :: "n"(N) : "memory"); }
__device__ __forceinline__ void wait_lgkm0() { asm volatile("s_waitcnt lgkmcnt(0)" ::: "memory"); }
__device__ __forceinline__ void acquire_agent() { asm volatile("buffer_inv sc1" ::: "memory"); }
__device__ __forceinline__ unsigned pack2(float a, float b) {
    bf16x2 v = {(__bf16)a, (__bf16)b};
    return __builtin_bit_cast(unsigned, v);
}
__device__ __forceinline__ void store16_dev(char* base, unsigned voff, u32x4 v) {
    asm volatile("global_store_dwordx4 %0, %1, %2 sc1\n\ts_nop 1" :: "v"(voff), "v"(v), "s"(base) : "memory");
}
__device__ __forceinline__ void store16(char* base, unsigned voff, u32x4 v) {
    asm volatile("global_store_dwordx4 %0, %1, %2\n\ts_nop 1" :: "v"(voff), "v"(v), "s"(base) : "memory");
}
__device__ __forceinline__ GAS u64* ctl_line(char* ws, int line) { return (GAS u64*)(ws + WS_CTL + (size_t)line * 128); }
__device__ __forceinline__ void flag_store(char* ws, int line, u64 tag) {
    __hip_atomic_store(ctl_line(ws, line), tag, __ATOMIC_RELAXED, __HIP_MEMORY_SCOPE_AGENT);
}
__device__ __forceinline__ u64 flag_load(char* ws, int line) {
    return __hip_atomic_load(ctl_line(ws, line), __ATOMIC_RELAXED, __HIP_MEMORY_SCOPE_AGENT);
}

struct T64State { f32x32 acc0, acc1; i32x16 fa0, fa1; };

__device__ __forceinline__ void t64_nt(f32x32& acc0, f32x32& acc1, i32x16& fm, i32x16& fn, unsigned& am0, unsigned& am1,
                                       unsigned& an0, unsigned& an1, int& slot, int n, int zero) {
    int dl;
    asm volatile(ASM_T64_NT
                 : "+{v[0:31]}"(acc0), "+{v[32:63]}"(acc1), "+{v[64:79]}"(fm), "+{v[80:95]}"(fn),
                   [am0] "+v"(am0), [am1] "+v"(am1), [an0] "+v"(an0), [an1] "+v"(an1),
                   [slot] "+s"(slot), [n] "+s"(n), [dl] "=&s"(dl)
                 : [z] "s"(zero), [dwrap] "s"(-98304), [dstep] "s"(32768)
                 : "v96", "v97", "v98", "v99", "v100", "v101", "v102", "v103", "v104", "v105", "v106", "v107",
                   "v108", "v109", "v110", "v111", "v112", "v113", "v114", "v115", "v116", "v117", "v118", "v119",
                   "v120", "v121", "v122", "v123", "v124", "v125", "v126", "v127", "scc", "memory");
}

template <bool DEV>
__device__ __forceinline__ void t64_store_perm(const f32x32& acc0, const f32x32& acc1, char* base, unsigned voff, unsigned rstride16,
                                               unsigned sign) {
#pragma unroll
    for (int i = 0; i < 4; ++i) {
#pragma unroll
        for (int J = 0; J < 2; ++J) {
            const int r = (i & 1) * 16 + J * 8;
            const f32x32& a = (i < 2) ? acc0 : acc1;
            u32x4 v;
            v[0] = pack2(a[r + 0], a[r + 1]) ^ sign;
            v[1] = pack2(a[r + 2], a[r + 3]) ^ sign;
            v[2] = pack2(a[r + 4], a[r + 5]) ^ sign;
            v[3] = pack2(a[r + 6], a[r + 7]) ^ sign;
            if (DEV) store16_dev(base, voff + i * rstride16 + J * 64, v);
            else store16(base, voff + i * rstride16 + J * 64, v);
        }
    }
}

__device__ __forceinline__ void spectral_consumer(int cw, int lane, char* ws, char* out, int eb, int sb, int tb, int eb3) {
    const int wm = cw >> 1, wn = cw & 1;
    const unsigned pos = (unsigned)(((lane >> 4) ^ ((lane & 15) >> 1)) * 16);
    unsigned am0 = (unsigned)((64 * wm + (lane & 15)) * 128) + pos;
    unsigned an0 = 16384u + (unsigned)((64 * wn + (lane & 15)) * 128) + pos;
    unsigned am1 = am0 ^ 64u, an1 = an0 ^ 64u;
    f32x32 acc0, acc1;
    i32x16 fm, fn;
    wg_barrier();
    asm volatile(
        "ds_read_b128 v[64:67], %2\n ds_read_b128 v[68:71], %2 offset:2048\n ds_read_b128 v[72:75], %2 offset:4096\n ds_read_b128 v[76:79], %2 offset:6144\n"
        "ds_read_b128 v[80:83], %3\n ds_read_b128 v[84:87], %3 offset:2048\n ds_read_b128 v[88:91], %3 offset:4096\n ds_read_b128 v[92:95], %3 offset:6144\n"
        "s_waitcnt lgkmcnt(0)\n"
        : "={v[64:79]}"(fm), "={v[80:95]}"(fn) : "v"(am0), "v"(an0) : "memory");
    asm volatile("" : "={v[0:31]}"(acc0), "={v[32:63]}"(acc1));
    int slot = 0;
    const unsigned g8 = (unsigned)(lane >> 4) * 16u;
#pragma unroll 1
    for (int seg = 0; seg < 5; ++seg) {
        constexpr u64 NSEG = 16ull | ((u64)DRAIN_STEPS << 8) | ((u64)(16 - DRAIN_STEPS) << 16) | ((u64)DRAIN_STEPS << 24) |
                             ((u64)(64 - DRAIN_STEPS) << 32);
        const int n = (int)((NSEG >> (8 * seg)) & 0xff);
        const int zero = (0xb >> seg) & 1;
        t64_nt(acc0, acc1, fm, fn, am0, am1, an0, an1, slot, n, zero);
        if (seg == 1 || seg == 3) {
            wait_vm<0>();
        } else if (seg != 4) {
            char* base = ws + WS_ABT + (size_t)(eb * 128 + 64 * wm) * 8192 + (size_t)(sb * 128 + 64 * wn) * 2 + (seg == 2 ? 4096 : 0);
            t64_store_perm<true>(acc0, acc1, base, (unsigned)(lane & 15) * 8192u + g8, 16u * 8192u, seg == 2 ? 0x80008000u : 0u);
        } else {
            char* base = out + (size_t)(tb * 128 + 64 * wm) * 4096 + (size_t)(1024 + eb3 * 128 + 64 * wn) * 2;
            t64_store_perm<false>(acc0, acc1, base, (unsigned)(lane & 15) * 4096u + g8, 16u * 4096u, 0u);
        }
    }
}

#define EXPM 0
#define NLOAD 4
#define ROT 0
__device__ __forceinline__ void spectral_loader(int lw, int lane, const char* Bx, const char* Cr, const char* Ci, const char* Dr,
                                                const char* Di, char* ws, int eb, int sb, int tb, int eb3, int x) {
    constexpr int PER = 16 / NLOAD;
    const unsigned swz = (unsigned)(((lane & 7) ^ (4 * (lw & 1) + (lane >> 4))) * 16);
    const unsigned mrow = (unsigned)(8 * lw + (lane >> 3));
    const unsigned nrow = (unsigned)(32 * (lw >> 2) + 8 * (2 * (lw & 1) + (lane >> 5)) + 4 * ((lw >> 1) & 1) + ((lane >> 3) & 3));
    const unsigned ldsw = (unsigned)lw * 1024u;
    int s = 0;
#pragma unroll 1
    for (int ph = 0; ph < 4; ++ph) {
        const unsigned ms = ph < 2 ? 2048u : 4096u, ns = ph < 2 ? 2048u : 8192u;
        const char* mp = (ph == 0 ? Dr : ph == 1 ? Di : ph == 2 ? Cr : Ci) + (size_t)(ph < 2 ? eb : tb) * 128 * ms;
        const char* np = ph < 2 ? Bx + (size_t)sb * 128 * 2048 : ws + WS_ABT + (size_t)eb3 * 128 * 8192 + (ph == 3 ? 4096 : 0);
        const i32x4 srdm = make_srd(mp, 128 * ms), srdn = make_srd(np, 128 * ns);
        const unsigned vm = mrow * ms + swz, vn = nrow * ns + swz;
        const int send = ph == 0 ? 16 : 32 * ph;
        const unsigned kmask = ph < 2 ? 2047u : 4095u;
        unsigned ko = (unsigned)(ROT * x * (ph < 2 ? 256 : 512)) & kmask;
#pragma unroll 1
        for (; s < send; ++s) {
            if (s >= 4) { wait_vm<4 * PER>(); wg_barrier(); }
            const unsigned lds = (unsigned)(s & 3) * 32768u + ldsw;
#pragma unroll
            for (int i = 0; i < PER; ++i) dma16(vm, srdm, ko + i * 8 * NLOAD * ms, lds + i * 1024u * NLOAD);
#pragma unroll
            for (int i = 0; i < PER; ++i) dma16(vn, srdn, ko + i * 8 * NLOAD * ns, lds + 16384u + i * 1024u * NLOAD);
            ko = (ko + 128) & kmask;
            if (s == 3) { wait_vm<6 * PER>(); wg_barrier(); }
        }
    }
    wait_vm<4 * PER>(); wg_barrier();
    wait_vm<2 * PER>(); wg_barrier();
    wait_vm<0>(); wg_barrier();
    wg_barrier();
}

struct Gate {
    int state;
};
__device__ __forceinline__ bool mail_match(unsigned lds, int lane, u64 tag) {
    u32x2 v;
    asm volatile("ds_read_b64 %0, %1\n\ts_waitcnt lgkmcnt(0)" : "=v"(v) : "v"(lds + (unsigned)lane * 16u) : "memory");
    const u64 x = ((u64)v[1] << 32) | v[0];
    return __all(x == tag);
}
__device__ __forceinline__ void mail_clear(unsigned lds, int lane, u64 tag) {
    u32x2 v = {~(unsigned)tag, ~(unsigned)(tag >> 32)};
    asm volatile("ds_write_b64 %0, %1\n\ts_waitcnt lgkmcnt(0)" :: "v"(lds + (unsigned)lane * 16u), "v"(v) : "memory");
}
__device__ __forceinline__ int gate_step(int state, int lane, i32x4 srdc, unsigned voff, u64 tag) {
    if (state == 1) {
        if (mail_match(LDS_MAIL, lane, tag)) {
            acquire_agent();
            dma16_dev(voff, srdc, 0, LDS_MAIL + 1024);
            state = 2;
        } else {
            dma16_dev(voff, srdc, 0, LDS_MAIL);
        }
    } else if (state == 2) {
        if (mail_match(LDS_MAIL + 1024, lane, tag)) { wait_vm<0>(); state = 3; }
    }
    return state;
}

__device__ __forceinline__ void spectral_control(int lane, char* ws, int w, int eb, int sb, int eb3) {
    u64 tag;
    {
        u32x2 e;
        GAS u64* ep = ctl_line(ws, CTL_EPOCH);
        asm volatile("global_load_dwordx2 %0, %1, off sc1" : "=v"(e) : "v"(ep) : "memory");
        wg_barrier();
        wg_barrier();
        wg_barrier();
        asm volatile("s_waitcnt vmcnt(0)" : "+v"(e) :: "memory");
        tag = (((u64)e[1] << 32) | e[0]) + 1;
    }
    if (lane == 0) flag_store(ws, CTL_ARR + w, tag);
    const i32x4 srdc = make_srd(ws + WS_CTL, 1024 * 128);
    int state = 0;
    unsigned voff = 0;
#pragma unroll 1
    for (int k = 2; k < 96; ++k) {
        if (k == 16 + DRAIN_STEPS + 1 && lane == 0) flag_store(ws, CTL_FA + eb * 16 + sb, tag);
        if (k == 32 + DRAIN_STEPS + 1 && lane == 0) flag_store(ws, CTL_FB + eb * 16 + sb, tag);
        if (k == 16 + DRAIN_STEPS + 2 || k == 40) {
            voff = (unsigned)(((k == 40 ? CTL_FB : CTL_FA) + eb3 * 16 + (lane & 15)) * 128);
            mail_clear(LDS_MAIL, lane, tag);
            mail_clear(LDS_MAIL + 1024, lane, tag);
            dma16_dev(voff, srdc, 0, LDS_MAIL);
            state = 1;
        } else if (state == 1 || state == 2) {
            state = gate_step(state, lane, srdc, voff, tag);
        }
        if (k == 28 || k == 60) {
            for (int spin = 0; spin < SPIN_LIMIT && state != 3; ++spin) {
                __builtin_amdgcn_s_sleep(2);
                state = gate_step(state, lane, srdc, voff, tag);
            }
            state = 0;
        }
        if (w == 0 && k == 70) {
            for (int spin = 0; spin < SPIN_LIMIT; ++spin) {
                bool ok = true;
#pragma unroll
                for (int q = 0; q < 4; ++q) ok = ok && (flag_load(ws, CTL_ARR + q * 64 + lane) == tag);
                if (__all(ok)) break;
                __builtin_amdgcn_s_sleep(4);
            }
            if (lane == 0) flag_store(ws, CTL_EPOCH, tag);
        }
        wg_barrier();
    }
}

__device__ __forceinline__ void pf4(unsigned voff, i32x4 srd, unsigned soff, unsigned lds) {
    asm volatile("s_mov_b32 m0, %3\n\ts_nop 0\n\tbuffer_load_dword %0, %1, %2 offen lds"
                 :: "v"(voff), "s"(srd), "s"(soff), "s"(lds) : "memory");
}
__device__ __forceinline__ void spectral_prefetch(int lane, const char* Bx, const char* Cr, const char* Ci, const char* Dr,
                                                  const char* Di, char* ws, int idx, int eb, int sb, int tb, int eb3) {
    const unsigned ja = (unsigned)(idx & 3), jb = (unsigned)(idx >> 2);
    const unsigned r = (unsigned)(lane & 31), half = (unsigned)(lane >> 5) * 128u;
    const unsigned vm_lo = (32 * jb + r) * 2048u + half, vn_lo = (32 * ja + r) * 2048u + half;
    const unsigned vm_hi = (32 * ja + r) * 4096u + half, vn_hi = (32 * jb + r) * 8192u + half;
    const i32x4 sdr = make_srd(Dr + (size_t)eb * 128 * 2048, 128 * 2048), sdi = make_srd(Di + (size_t)eb * 128 * 2048, 128 * 2048);
    const i32x4 scr = make_srd(Cr + (size_t)tb * 128 * 4096, 128 * 4096), sci = make_srd(Ci + (size_t)tb * 128 * 4096, 128 * 4096);
    const i32x4 sx = make_srd(Bx + (size_t)sb * 128 * 2048, 128 * 2048);
    const i32x4 sab = make_srd(ws + WS_ABT + (size_t)eb3 * 128 * 8192, 128 * 8192);
    int nm = 4, nn = 4;
#pragma unroll 1
    for (int k = -1; k < 96; ++k) {
        while (nm <= k + PF_DIST && nm < 96) {
            if (nm < 16) pf4(vm_lo, sdr, nm * 128, LDS_MAIL + 2048);
            else if (nm < 32) pf4(vm_lo, sdi, (nm - 16) * 128, LDS_MAIL + 2048);
            else if (nm < 64) pf4(vm_hi, scr, (nm - 32) * 128, LDS_MAIL + 2048);
            else pf4(vm_hi, sci, (nm - 64) * 128, LDS_MAIL + 2048);
            nm += 2;
        }
        const int lim = k < 28 ? 32 : k < 60 ? 64 : 96;
        while (nn <= k + PF_DIST - 1 && nn < lim) {
            if (nn < 16) pf4(vn_lo, sx, nn * 128, LDS_MAIL + 2048);
            else if (nn >= 32 && nn > k + 4) pf4(vn_hi, sab, (nn - 32) * 128, LDS_MAIL + 2048);
            nn += 2;
        }
        if (k == -1) wg_barrier();
        if (k < 95) wg_barrier();
    }
    wait_vm<0>();
}

static constexpr unsigned LDS_NRING = 96 * 1024;
static constexpr unsigned LDS_PREV = 64 * 1024;
static constexpr unsigned LDS_WORDS = 156 * 1024;
static constexpr unsigned LDS_TAG = LDS_WORDS, LDS_CNT = LDS_WORDS + 8, LDS_RDY_KV = LDS_WORDS + 12, LDS_RDY_O = LDS_WORDS + 16;
static constexpr float Q_SCALE = 0.17677669529663687f * 1.4426950408889634f;

__device__ __forceinline__ f32x4 mfma16(bf16x8 a, bf16x8 b, f32x4 c) { return __builtin_amdgcn_mfma_f32_16x16x32_bf16(a, b, c, 0, 0, 0); }
__device__ __forceinline__ bf16x8 lds_read16(unsigned a) { return *(LAS bf16x8*)(size_t)a; }
__device__ __forceinline__ bf16x4 lds_read_tr(unsigned a) { return __builtin_amdgcn_ds_read_tr16_b64_v4bf16((LAS bf16x4*)(size_t)a); }
__device__ __forceinline__ void lds_write16(unsigned a, u32x4 v) { *(LAS u32x4*)(size_t)a = v; }
__device__ __forceinline__ unsigned lds_word(unsigned a) { return *(volatile LAS unsigned*)(size_t)a; }
__device__ __forceinline__ void lds_set(unsigned a, unsigned v) { *(volatile LAS unsigned*)(size_t)a = v; }
__device__ __forceinline__ u64 lds_tag() {
    const u32x2 v = *(volatile LAS u32x2*)(size_t)LDS_TAG;
    return ((u64)v[1] << 32) | v[0];
}
__device__ __forceinline__ void swap16(unsigned& a, unsigned& b) {
    auto r = __builtin_amdgcn_permlane16_swap(a, b, false, false);
    a = r[0]; b = r[1];
}
__device__ __forceinline__ float row_max(float x) {
    auto r = __builtin_amdgcn_permlane32_swap(__float_as_uint(x), __float_as_uint(x), false, false);
    x = __builtin_fmaxf(__uint_as_float(r[0]), __uint_as_float(r[1]));
    auto t = __builtin_amdgcn_permlane16_swap(__float_as_uint(x), __float_as_uint(x), false, false);
    return __builtin_fmaxf(__uint_as_float(t[0]), __uint_as_float(t[1]));
}
__device__ __forceinline__ float max4(float m, f32x4 s) {
    return __builtin_fmaxf(__builtin_fmaxf(__builtin_fmaxf(__builtin_fmaxf(m, s[0]), s[1]), s[2]), s[3]);
}
__device__ __forceinline__ bf16x8 as_frag(u32x4 v) { return __builtin_bit_cast(bf16x8, v); }

__device__ __forceinline__ void qkv_gemm(f32x32& a0, f32x32& a1, f32x32& a2, unsigned am0, unsigned am1, unsigned an0, unsigned an1) {
    int ms = 0, ns = 0, n = 16, dm, dn;
    asm volatile(ASM_QKV
                 : "={v[0:31]}"(a0), "={v[32:63]}"(a1), "={v[64:95]}"(a2),
                   [am0] "+v"(am0), [am1] "+v"(am1), [an0] "+v"(an0), [an1] "+v"(an1),
                   [ms] "+s"(ms), [ns] "+s"(ns), [n] "+s"(n), [dm] "=&s"(dm), [dn] "=&s"(dn)
                 : [mwrap] "s"(-65536), [mstep] "s"(32768), [nwrap] "s"(-24576), [nstep] "s"(24576)
                 : "v96", "v97", "v98", "v99", "v100", "v101", "v102", "v103", "v104", "v105", "v106", "v107",
                   "v108", "v109", "v110", "v111", "v112", "v113", "v114", "v115", "v116", "v117", "v118", "v119",
                   "v120", "v121", "v122", "v123", "v124", "v125", "v126", "v127", "v128", "v129", "v130", "v131",
                   "v132", "v133", "v134", "v135", "v136", "v137", "v138", "v139", "v140", "v141", "v142", "v143",
                   "v144", "v145", "v146", "v147", "v148", "v149", "v150", "v151", "scc", "memory");
}

struct AttnState {
    bf16x8 q[4];
    f32x4 o[4][2];
    f32x4 l[4];
    float m[4];
};

template <bool PREV>
__device__ __forceinline__ void attn_max(const AttnState& st, float (&mx)[4], unsigned ka, int wm, f32x4 bias) {
    const f32x4 zero = {0.f, 0.f, 0.f, 0.f};
    const int first = PREV ? 4 * wm + 4 : 0, count = PREV ? 12 - 4 * wm : 4 * wm;
    unsigned a = ka + (unsigned)first * 1024u;
#pragma unroll 1
    for (int it = 0; it < count; it += 2, a += 2048u) {
        const bf16x8 k0 = lds_read16(a), k1 = lds_read16(a + 1024u);
#pragma unroll
        for (int qb = 0; qb < 4; ++qb) {
            mx[qb] = max4(mx[qb], mfma16(k0, st.q[qb], zero));
            mx[qb] = max4(mx[qb], mfma16(k1, st.q[qb], zero));
        }
    }
    a = ka + (unsigned)wm * 4096u;
#pragma unroll
    for (int t = 0; t < 4; ++t) {
        const bf16x8 k = lds_read16(a + t * 1024u);
#pragma unroll
        for (int qb = 0; qb < 4; ++qb) {
            if (t == qb) mx[qb] = max4(mx[qb], mfma16(k, st.q[qb], bias));
            else if (PREV ? t > qb : t < qb) mx[qb] = max4(mx[qb], mfma16(k, st.q[qb], zero));
        }
    }
}

__device__ __forceinline__ u32x2 exp_pack(f32x4 s) {
    u32x2 r;
    r[0] = pack2(__builtin_amdgcn_exp2f(s[0]), __builtin_amdgcn_exp2f(s[1]));
    r[1] = pack2(__builtin_amdgcn_exp2f(s[2]), __builtin_amdgcn_exp2f(s[3]));
    return r;
}
struct VFrag { bf16x8 d0, d1; };
__device__ __forceinline__ VFrag read_v(unsigned va0, unsigned va1, unsigned off) {
    VFrag v;
    const bf16x4 a = lds_read_tr(va0 + off), b = lds_read_tr(va0 + off + 1024u);
    const bf16x4 c = lds_read_tr(va1 + off), d = lds_read_tr(va1 + off + 1024u);
    v.d0 = __builtin_shufflevector(a, b, 0, 1, 2, 3, 4, 5, 6, 7);
    v.d1 = __builtin_shufflevector(c, d, 0, 1, 2, 3, 4, 5, 6, 7);
    return v;
}
template <int MODE0, int MODE1>
__device__ __forceinline__ void attn_pair(AttnState& st, int qb, bf16x8 k0, bf16x8 k1, const VFrag& v, f32x4 bias, bf16x8 ones) {
    const float nm = -st.m[qb];
    const f32x4 c = {nm, nm, nm, nm};
    u32x2 p0 = {0u, 0u}, p1 = {0u, 0u};
    if (MODE0) p0 = exp_pack(mfma16(k0, st.q[qb], MODE0 == 2 ? c + bias : c));
    if (MODE1) p1 = exp_pack(mfma16(k1, st.q[qb], MODE1 == 2 ? c + bias : c));
    const u32x4 pp = {p0[0], p0[1], p1[0], p1[1]};
    const bf16x8 p = as_frag(pp);
    st.o[qb][0] = mfma16(v.d0, p, st.o[qb][0]);
    st.o[qb][1] = mfma16(v.d1, p, st.o[qb][1]);
    st.l[qb] = mfma16(ones, p, st.l[qb]);
}
template <bool PREV>
__device__ __forceinline__ void attn_acc(AttnState& st, unsigned ka, unsigned va0, unsigned va1, int wm, f32x4 bias, bf16x8 ones) {
    const int first = PREV ? 4 * wm + 4 : 0, count = PREV ? 12 - 4 * wm : 4 * wm;
    unsigned off = (unsigned)first * 1024u;
#pragma unroll 1
    for (int it = 0; it < count; it += 2, off += 2048u) {
        const bf16x8 k0 = lds_read16(ka + off), k1 = lds_read16(ka + off + 1024u);
        const VFrag v = read_v(va0, va1, off);
#pragma unroll
        for (int qb = 0; qb < 4; ++qb) attn_pair<1, 1>(st, qb, k0, k1, v, bias, ones);
    }
    off = (unsigned)wm * 4096u;
    {
        const bf16x8 k0 = lds_read16(ka + off), k1 = lds_read16(ka + off + 1024u);
        const VFrag v = read_v(va0, va1, off);
        if (PREV) {
            attn_pair<2, 1>(st, 0, k0, k1, v, bias, ones);
            attn_pair<0, 2>(st, 1, k0, k1, v, bias, ones);
        } else {
            attn_pair<2, 0>(st, 0, k0, k1, v, bias, ones);
            attn_pair<1, 2>(st, 1, k0, k1, v, bias, ones);
            attn_pair<1, 1>(st, 2, k0, k1, v, bias, ones);
            attn_pair<1, 1>(st, 3, k0, k1, v, bias, ones);
        }
    }
    off += 2048u;
    {
        const bf16x8 k0 = lds_read16(ka + off), k1 = lds_read16(ka + off + 1024u);
        const VFrag v = read_v(va0, va1, off);
        if (PREV) {
            attn_pair<1, 1>(st, 0, k0, k1, v, bias, ones);
            attn_pair<1, 1>(st, 1, k0, k1, v, bias, ones);
            attn_pair<2, 1>(st, 2, k0, k1, v, bias, ones);
            attn_pair<0, 2>(st, 3, k0, k1, v, bias, ones);
        } else {
            attn_pair<2, 0>(st, 2, k0, k1, v, bias, ones);
            attn_pair<1, 2>(st, 3, k0, k1, v, bias, ones);
        }
    }
}

__device__ __forceinline__ void t64_tr(f32x32& acc0, f32x32& acc1, i32x16& fm, i32x16& fn, unsigned am0, unsigned am1,
                                       unsigned an0, unsigned an1, unsigned an2, unsigned an3) {
    int dl, slot = 0, n = 16;
    asm volatile(ASM_T64_TR
                 : "={v[0:31]}"(acc0), "={v[32:63]}"(acc1), "+{v[64:79]}"(fm), "+{v[80:95]}"(fn),
                   [am0] "+v"(am0), [am1] "+v"(am1), [an0] "+v"(an0), [an1] "+v"(an1), [an2] "+v"(an2), [an3] "+v"(an3),
                   [slot] "+s"(slot), [n] "+s"(n), [dl] "=&s"(dl)
                 : [z] "s"(1), [dwrap] "s"(-98304), [dstep] "s"(32768)
                 : "v96", "v97", "v98", "v99", "v100", "v101", "v102", "v103", "v104", "v105", "v106", "v107",
                   "v108", "v109", "v110", "v111", "v112", "v113", "v114", "v115", "v116", "v117", "v118", "v119",
                   "v120", "v121", "v122", "v123", "v124", "v125", "v126", "v127", "scc", "memory");
}

__device__ __forceinline__ void attn_consumer(int cw, int lane, char* ws, char* out, int w, int ib, int p) {
    const int wn = cw & 1, wm = cw < 4 ? (cw >> 1) : 3 - ((cw - 4) >> 1);
    if (cw == 0) {
        const u64 tag = flag_load(ws, CTL_EPOCH) + 1;
        if (lane == 0) {
            flag_store(ws, CTL_ARR + w, tag);
            *(volatile LAS u32x2*)(size_t)LDS_TAG = (u32x2){(unsigned)tag, (unsigned)(tag >> 32)};
            lds_set(LDS_CNT, 0u); lds_set(LDS_RDY_KV, 0u); lds_set(LDS_RDY_O, 0u);
        }
        wait_lgkm0();
    }
    AttnState st;
    int lr = lane & 15, g = lane >> 4, q4 = (lane >> 2) & 3, p4 = lane & 3;
    unsigned koff;
    {
        const unsigned am0 = (unsigned)((64 * wm + lr) * 128 + ((g ^ (lr >> 1)) * 16));
        const unsigned nsw = (unsigned)(((q4 >> 1) & 1) | ((g & 1) << 1));
        const unsigned anb = LDS_NRING + (unsigned)((8 * g + q4) * 128 + p4 * 8);
        const unsigned an0 = anb + (((unsigned)(2 * wn) ^ nsw) * 32u), an1 = anb + (((unsigned)(2 * wn + 1) ^ nsw) * 32u);
        f32x32 a0, a1, a2;
        wg_barrier();
        qkv_gemm(a0, a1, a2, am0, am0 ^ 64u, an0, an1);
        lane = fresh(lane);
        lr = lane & 15; g = lane >> 4; q4 = (lane >> 2) & 3; p4 = lane & 3;
        koff = (unsigned)(lr * 64 + ((g ^ ((-(lr >> 2)) & 3)) * 16));
#define ACC(i, j, r) ((24 * (i) + 4 * (j) + (r)) < 32 ? a0[(24 * (i) + 4 * (j) + (r)) & 31] : (24 * (i) + 4 * (j) + (r)) < 64 ? a1[(24 * (i) + 4 * (j) + (r)) & 31] : a2[(24 * (i) + 4 * (j) + (r)) & 31])
        const unsigned kw = (unsigned)(wn * 16384 + wm * 4096) + koff;
        const unsigned vw = (unsigned)(32768 + wn * 16384 + wm * 4096 + lr * 64 + ((((g & 1) ^ ((lr >> 2) & 1)) * 2 + (g >> 1)) * 16));
#pragma unroll
        for (int i = 0; i < 4; ++i) {
            u32x4 k = {pack2(ACC(i, 2, 0), ACC(i, 2, 1)), pack2(ACC(i, 2, 2), ACC(i, 2, 3)),
                       pack2(ACC(i, 3, 0), ACC(i, 3, 1)), pack2(ACC(i, 3, 2), ACC(i, 3, 3))};
            lds_write16(kw + i * 1024u, k);
            unsigned x0 = pack2(ACC(i, 4, 0), ACC(i, 4, 1)), x1 = pack2(ACC(i, 4, 2), ACC(i, 4, 3));
            unsigned y0 = pack2(ACC(i, 5, 0), ACC(i, 5, 1)), y1 = pack2(ACC(i, 5, 2), ACC(i, 5, 3));
            swap16(x0, y0); swap16(x1, y1);
            lds_write16(vw + i * 1024u, (u32x4){x0, x1, y0, y1});
            st.q[i] = as_frag((u32x4){pack2(ACC(i, 0, 0) * Q_SCALE, ACC(i, 0, 1) * Q_SCALE), pack2(ACC(i, 0, 2) * Q_SCALE, ACC(i, 0, 3) * Q_SCALE),
                                      pack2(ACC(i, 1, 0) * Q_SCALE, ACC(i, 1, 1) * Q_SCALE), pack2(ACC(i, 1, 2) * Q_SCALE, ACC(i, 1, 3) * Q_SCALE)});
        }
#undef ACC
    }
    wait_lgkm0();
    wg_barrier();
    const float ninf = -__builtin_inff();
    f32x4 bias_own, bias_prev;
#pragma unroll
    for (int r = 0; r < 4; ++r) {
        const bool le = 4 * g + r <= lr;
        bias_own[r] = le ? 0.f : ninf;
        bias_prev[r] = le ? ninf : 0.f;
    }
    const bf16x8 ones = as_frag((u32x4){0x3f803f80u, 0x3f803f80u, 0x3f803f80u, 0x3f803f80u});
    const unsigned ka = (unsigned)(wn * 16384) + koff;
    const unsigned vb = (unsigned)(32768 + wn * 16384 + (4 * g + q4) * 64 + p4 * 8);
    const unsigned va0 = vb + (unsigned)((g & 1) * 32), va1 = vb + (unsigned)(((g & 1) ^ 1) * 32);
    {
        float mx[4] = {ninf, ninf, ninf, ninf};
        attn_max<false>(st, mx, ka, wm, bias_own);
#pragma unroll
        for (int qb = 0; qb < 4; ++qb) {
            st.m[qb] = row_max(mx[qb]);
            st.o[qb][0] = st.o[qb][1] = st.l[qb] = (f32x4){0.f, 0.f, 0.f, 0.f};
        }
        attn_acc<false>(st, ka, va0, va1, wm, bias_own, ones);
    }
    wg_barrier();
    if (ib > 0) {
        float mx[4] = {st.m[0], st.m[1], st.m[2], st.m[3]};
        attn_max<true>(st, mx, ka + LDS_PREV, wm, bias_prev);
#pragma unroll
        for (int qb = 0; qb < 4; ++qb) {
            const float mn = row_max(mx[qb]);
            const float al = __builtin_amdgcn_exp2f(st.m[qb] - mn);
            st.m[qb] = mn;
            st.o[qb][0] *= al; st.o[qb][1] *= al; st.l[qb] *= al;
        }
        attn_acc<true>(st, ka + LDS_PREV, va0 + LDS_PREV, va1 + LDS_PREV, wm, bias_prev, ones);
    }
    lane = fresh(lane);
    lr = lane & 15; g = lane >> 4; q4 = (lane >> 2) & 3; p4 = lane & 3;
    {
        char* ob = ws + WS_O + (size_t)(ib * 16 + p) * 32768;
#pragma unroll
        for (int qb = 0; qb < 4; ++qb) {
            const float inv = 1.0f / st.l[qb][0];
            const int row = 64 * wm + 16 * qb + lr;
            u32x4 v = {pack2(st.o[qb][0][0] * inv, st.o[qb][0][1] * inv), pack2(st.o[qb][0][2] * inv, st.o[qb][0][3] * inv),
                       pack2(st.o[qb][1][0] * inv, st.o[qb][1][1] * inv), pack2(st.o[qb][1][2] * inv, st.o[qb][1][3] * inv)};
            store16_dev(ob, (unsigned)(row * 128 + (((4 * wn + g) ^ ((row >> 1) & 7)) * 16)), v);
        }
        wait_vm<0>();
    }
    wg_barrier();
    if (cw >= 4) return;
    {
        const int om = cw >> 1, on = cw & 1;
        const unsigned am0 = (unsigned)((64 * om + lr) * 128 + ((g ^ (lr >> 1)) * 16));
        const unsigned anb = 16384u + (unsigned)((8 * g + q4) * 256 + p4 * 8);
        const unsigned nsw = (unsigned)(q4 | ((g & 1) << 2));
        const unsigned an0 = anb + (((unsigned)(4 * on + 0) ^ nsw) * 32u), an1 = anb + (((unsigned)(4 * on + 1) ^ nsw) * 32u);
        const unsigned an2 = anb + (((unsigned)(4 * on + 2) ^ nsw) * 32u), an3 = anb + (((unsigned)(4 * on + 3) ^ nsw) * 32u);
        f32x32 acc0, acc1;
        i32x16 fm, fn;
        wg_barrier();
        asm volatile(
            "ds_read_b128 v[64:67], %2\n ds_read_b128 v[68:71], %2 offset:2048\n ds_read_b128 v[72:75], %2 offset:4096\n ds_read_b128 v[76:79], %2 offset:6144\n"
            "ds_read_b64_tr_b16 v[80:81], %3\n ds_read_b64_tr_b16 v[82:83], %3 offset:1024\n"
            "ds_read_b64_tr_b16 v[84:85], %4\n ds_read_b64_tr_b16 v[86:87], %4 offset:1024\n"
            "ds_read_b64_tr_b16 v[88:89], %5\n ds_read_b64_tr_b16 v[90:91], %5 offset:1024\n"
            "ds_read_b64_tr_b16 v[92:93], %6\n ds_read_b64_tr_b16 v[94:95], %6 offset:1024\n"
            "s_waitcnt lgkmcnt(0)\n"
            : "={v[64:79]}"(fm), "={v[80:95]}"(fn) : "v"(am0), "v"(an0), "v"(an1), "v"(an2), "v"(an3) : "memory");
        t64_tr(acc0, acc1, fm, fn, am0, am0 ^ 64u, an0, an1, an2, an3);
        char* ob = out + (size_t)(256 * ib + 128 * (p & 1) + 64 * om) * 4096 + (size_t)(128 * (p >> 1) + 64 * on) * 2;
        const unsigned vo = (unsigned)(lr * 4096 + ((g & 1) * 2 + (g >> 1)) * 16);
#pragma unroll
        for (int i = 0; i < 4; ++i) {
#pragma unroll
            for (int J = 0; J < 2; ++J) {
                const int r = (i & 1) * 16 + J * 8;
                const f32x32& a = (i < 2) ? acc0 : acc1;
                unsigned x0 = pack2(a[r + 0], a[r + 1]), x1 = pack2(a[r + 2], a[r + 3]);
                unsigned y0 = pack2(a[r + 4], a[r + 5]), y1 = pack2(a[r + 6], a[r + 7]);
                swap16(x0, y0); swap16(x1, y1);
                store16(ob, vo + i * 16u * 4096u + J * 64u, (u32x4){x0, x1, y0, y1});
            }
        }
    }
}

__device__ __forceinline__ void lds_wait_word(unsigned a) {
    for (int spin = 0; spin < SPIN_LIMIT && lds_word(a) == 0u; ++spin) __builtin_amdgcn_s_sleep(1);
}

__device__ __forceinline__ void attn_loader(int lw, int lane, const char* Ax, const char* Wqkv, const char* Wo, char* ws, int ib, int p) {
    {
        const unsigned swz = (unsigned)(((lane & 7) ^ (4 * (lw & 1) + (lane >> 4))) * 16);
        const unsigned vm = (unsigned)(8 * lw + (lane >> 3)) * 2048u + swz;
        const int r8 = lane >> 3, P = lane & 7;
        const unsigned c = (unsigned)((P >> 1) ^ (((r8 >> 1) & 1) | ((lw & 1) << 1)));
        const unsigned vn = (unsigned)(8 * lw + r8) * 6144u + c * 32u + (unsigned)(P & 1) * 16u;
        const i32x4 srdm = make_srd(Ax + (size_t)ib * 256 * 2048, 256 * 2048);
        const i32x4 srdn = make_srd(Wqkv + (size_t)p * 128, 1024 * 6144 - p * 128);
        const unsigned ldsw = (unsigned)lw * 1024u;
        auto issue_m = [&](int k) {
            const unsigned lds = (unsigned)(k % 3) * 32768u + ldsw;
#pragma unroll
            for (int i = 0; i < 8; ++i) dma16(vm, srdm, (unsigned)k * 128u + i * 32u * 2048u, lds + i * 4096u);
        };
        auto issue_n = [&](int k) {
            const unsigned lds = LDS_NRING + (unsigned)(k & 1) * 24576u + ldsw;
#pragma unroll
            for (int s = 0; s < 3; ++s)
#pragma unroll
                for (int i = 0; i < 2; ++i)
                    dma16(vn, srdn, (unsigned)k * 64u * 6144u + i * 32u * 6144u + s * 2048u, lds + s * 8192u + i * 4096u);
        };
        issue_n(0); issue_m(0); issue_n(1); issue_m(1); issue_m(2);
        wait_vm<22>();
        wg_barrier();
#pragma unroll 1
        for (int k = 0; k < 16; ++k) {
            if (k < 14) wait_vm<8>(); else wait_vm<0>();
            wg_barrier();
            if (k + 2 < 16) issue_n(k + 2);
            if (k + 3 < 16) issue_m(k + 3);
        }
    }
    wg_barrier();
    const u64 tag = lds_tag();
    if (ib < 7) {
        char* img = ws + WS_KV + (size_t)(ib * 16 + p) * 65536;
        const unsigned off = (unsigned)lw * 16384u + (unsigned)lane * 16u;
        u32x4 tmp[16];
#pragma unroll
        for (int i = 0; i < 16; ++i) tmp[i] = *(LAS u32x4*)(size_t)(off + i * 1024u);
#pragma unroll
        for (int i = 0; i < 16; ++i) store16_dev(img, off + i * 1024u, tmp[i]);
        wait_vm<0>();
        if (lane == 0) {
            const unsigned old = __hip_atomic_fetch_add((LAS unsigned*)(size_t)LDS_CNT, 1u, __ATOMIC_RELAXED, __HIP_MEMORY_SCOPE_WORKGROUP);
            if (old == 3u) flag_store(ws, CTL_KV + ib * 16 + p, tag);
        }
    }
    if (ib > 0) {
        if (lw == 0) {
            for (int spin = 0; spin < SPIN_LIMIT && flag_load(ws, CTL_KV + (ib - 1) * 16 + p) != tag; ++spin) __builtin_amdgcn_s_sleep(2);
            lds_set(LDS_RDY_KV, 1u);
        } else {
            lds_wait_word(LDS_RDY_KV);
        }
        const i32x4 srdk = make_srd(ws + WS_KV + (size_t)((ib - 1) * 16 + p) * 65536, 65536);
        const unsigned off = (unsigned)lw * 16384u;
#pragma unroll
        for (int i = 0; i < 16; ++i) dma16_dev((unsigned)lane * 16u, srdk, off + i * 1024u, LDS_PREV + off + i * 1024u);
        wait_vm<0>();
    }
    wg_barrier();
    wg_barrier();
    if (lw == 0 && lane == 0) flag_store(ws, CTL_FO + ib * 16 + p, tag);
    {
        const int r = lane >> 4, P = lane & 15;
        const unsigned c = (unsigned)((P >> 1) ^ (r | (((lw >> 1) & 1) << 2)));
        const unsigned vn = (unsigned)(16 * (lw & 1) + 4 * (lw >> 1) + r) * 2048u + c * 32u + (unsigned)(P & 1) * 16u;
        const i32x4 srdn = make_srd(Wo + (size_t)(p >> 1) * 256, 1024 * 2048 - (p >> 1) * 256);
        const i32x4 srdm = make_srd(ws + WS_O + (size_t)ib * 16 * 32768 + (size_t)(p & 1) * 16384, 16 * 32768 - (p & 1) * 16384);
        const unsigned ldsw = (unsigned)lw * 1024u, vm = (unsigned)lane * 16u + ldsw;
        auto issue_n = [&](int k) {
            const unsigned lds = (unsigned)(k & 3) * 32768u + 16384u + ldsw;
#pragma unroll
            for (int i = 0; i < 4; ++i)
                dma16(vn, srdn, ((unsigned)k * 64u + 32u * (i >> 1) + 8u * (i & 1)) * 2048u, lds + i * 4096u);
        };
        auto issue_m = [&](int k) {
            const unsigned lds = (unsigned)(k & 3) * 32768u + ldsw;
#pragma unroll
            for (int i = 0; i < 4; ++i) dma16_dev(vm, srdm, (unsigned)k * 32768u + i * 4096u, lds + i * 4096u);
        };
#pragma unroll
        for (int k = 0; k < 4; ++k) issue_n(k);
        if (lw == 0) {
            for (int spin = 0; spin < SPIN_LIMIT; ++spin) {
                if (__all(flag_load(ws, CTL_FO + ib * 16 + (lane & 15)) == tag)) break;
                __builtin_amdgcn_s_sleep(2);
            }
            lds_set(LDS_RDY_O, 1u);
        } else {
            lds_wait_word(LDS_RDY_O);
        }
#pragma unroll
        for (int k = 0; k < 4; ++k) issue_m(k);
        wait_vm<12>();
        wg_barrier();
#pragma unroll 1
        for (int k = 0; k < 16; ++k) {
            if (k == 0) wait_vm<8>(); else if (k == 1) wait_vm<12>(); else if (k < 13) wait_vm<16>(); else if (k == 13) wait_vm<8>(); else wait_vm<0>();
            wg_barrier();
            if (k + 4 < 16) { issue_n(k + 4); issue_m(k + 4); }
        }
    }
}

__global__ __launch_bounds__(768) void fused_kernel(const char* __restrict__ Ax, const char* __restrict__ Wqkv, const char* __restrict__ Wo,
                                                    const char* __restrict__ Bx, const char* __restrict__ Cr, const char* __restrict__ Ci,
                                                    const char* __restrict__ Dr, const char* __restrict__ Di, char* out, char* ws) {
    const int tid = threadIdx.x, lane = tid & 63, wid = uni(tid >> 6);
    const int w = blockIdx.x, x = w & 7, idx = w >> 3;
    if (idx < 16) {
        const int eb = 4 * (x & 1) + (idx & 3), sb = 4 * (x >> 1) + (idx >> 2);
        const int tb = sb, eb3 = eb;
        if (wid < 4) spectral_consumer(wid, lane, ws, out, eb, sb, tb, eb3);
        else if (wid < 4 + NLOAD) spectral_loader(wid - 4, lane, Bx, Cr, Ci, Dr, Di, ws, eb, sb, tb, eb3, x);
        else if (wid == 8) spectral_control(lane, ws, w, eb, sb, eb3);
        else if (wid == 9 && PF_DIST > 0) spectral_prefetch(lane, Bx, Cr, Ci, Dr, Di, ws, idx, eb, sb, tb, eb3);
    } else {
        const int a = idx - 16, ib = 4 * (x & 1) + (a & 3), p = 4 * (x >> 1) + (a >> 2);
        if (wid < 8) attn_consumer(wid, lane, ws, out, w, ib, p);
        else attn_loader(wid - 8, lane, Ax, Wqkv, Wo, ws, ib, p);
    }
}

extern "C" void kernel_launch(void* const* d_in, const int* in_sizes, int n_in, void* d_out, int out_size, void* d_ws, size_t ws_size,
                              hipStream_t stream) {
    (void)in_sizes; (void)n_in; (void)out_size; (void)ws_size;
    (void)hipFuncSetAttribute((const void*)fused_kernel, hipFuncAttributeMaxDynamicSharedMemorySize, LDS_BYTES);
    hipLaunchKernelGGL(fused_kernel, dim3(256), dim3(768), LDS_BYTES, stream, (const char*)d_in[0], (const char*)d_in[1],
                       (const char*)d_in[2], (const char*)d_in[3], (const char*)d_in[4], (const char*)d_in[5], (const char*)d_in[6],
                       (const char*)d_in[7], (char*)d_out, (char*)d_ws);
}
```

```cpp
#include <hip/hip_runtime.h>
#include <stdint.h>

#define GAS __attribute__((address_space(1)))
#define LAS __attribute__((address_space(3)))
typedef float f32x4 __attribute__((ext_vector_type(4)));
typedef float f32x32 __attribute__((ext_vector_type(32)));
typedef int i32x4 __attribute__((ext_vector_type(4)));
typedef int i32x8 __attribute__((ext_vector_type(8)));
typedef int i32x16 __attribute__((ext_vector_type(16)));
typedef unsigned u32x2 __attribute__((ext_vector_type(2)));
typedef unsigned u32x4 __attribute__((ext_vector_type(4)));
typedef __bf16 bf16x2 __attribute__((ext_vector_type(2)));
typedef __bf16 bf16x4 __attribute__((ext_vector_type(4)));
typedef __bf16 bf16x8 __attribute__((ext_vector_type(8)));
typedef unsigned long long u64;

#define ASM_T64_NT \
    "s_cmp_eq_u32 %[z], 0\n" \
    "s_cbranch_scc1 TOP%=\n" \
    "s_waitcnt lgkmcnt(0)\n" \
    "v_mfma_f32_16x16x32_bf16 v[0:3], v[80:83], v[64:67], 0\n" \
    "v_mfma_f32_16x16x32_bf16 v[4:7], v[84:87], v[64:67], 0\n" \
    "ds_read_b128 v[112:115], %[an1] offset:0\n" \
    "v_mfma_f32_16x16x32_bf16 v[8:11], v[88:91], v[64:67], 0\n" \
    "ds_read_b128 v[96:99], %[am1] offset:0\n" \
    "v_mfma_f32_16x16x32_bf16 v[12:15], v[92:95], v[64:67], 0\n" \
    "v_mfma_f32_16x16x32_bf16 v[16:19], v[80:83], v[68:71], 0\n" \
    "ds_read_b128 v[116:119], %[an1] offset:2048\n" \
    "v_mfma_f32_16x16x32_bf16 v[20:23], v[84:87], v[68:71], 0\n" \
    "ds_read_b128 v[100:103], %[am1] offset:2048\n" \
    "v_mfma_f32_16x16x32_bf16 v[24:27], v[88:91], v[68:71], 0\n" \
    "v_mfma_f32_16x16x32_bf16 v[28:31], v[92:95], v[68:71], 0\n" \
    "ds_read_b128 v[120:123], %[an1] offset:4096\n" \
    "v_mfma_f32_16x16x32_bf16 v[32:35], v[80:83], v[72:75], 0\n" \
    "ds_read_b128 v[104:107], %[am1] offset:4096\n" \
    "v_mfma_f32_16x16x32_bf16 v[36:39], v[84:87], v[72:75], 0\n" \
    "v_mfma_f32_16x16x32_bf16 v[40:43], v[88:91], v[72:75], 0\n" \
    "ds_read_b128 v[124:127], %[an1] offset:6144\n" \
    "v_mfma_f32_16x16x32_bf16 v[44:47], v[92:95], v[72:75], 0\n" \
    "ds_read_b128 v[108:111], %[am1] offset:6144\n" \
    "v_mfma_f32_16x16x32_bf16 v[48:51], v[80:83], v[76:79], 0\n" \
    "v_mfma_f32_16x16x32_bf16 v[52:55], v[84:87], v[76:79], 0\n" \
    "s_add_u32 %[slot], %[slot], 1\n" \
    "s_and_b32 %[slot], %[slot], 3\n" \
    "s_cmp_eq_u32 %[slot], 0\n" \
    "s_cselect_b32 %[dl], %[dwrap], %[dstep]\n" \
    "v_mfma_f32_16x16x32_bf16 v[56:59], v[88:91], v[76:79], 0\n" \
    "v_add_u32 %[am0], %[dl], %[am0]\n" \
    "v_add_u32 %[an0], %[dl], %[an0]\n" \
    "v_mfma_f32_16x16x32_bf16 v[60:63], v[92:95], v[76:79], 0\n" \
    "s_branch MID%=\n" \
    "TOP%=:\n" \
    "s_waitcnt lgkmcnt(0)\n" \
    "v_mfma_f32_16x16x32_bf16 v[0:3], v[80:83], v[64:67], v[0:3]\n" \
    "v_mfma_f32_16x16x32_bf16 v[4:7], v[84:87], v[64:67], v[4:7]\n" \
    "ds_read_b128 v[112:115], %[an1] offset:0\n" \
    "v_mfma_f32_16x16x32_bf16 v[8:11], v[88:91], v[64:67], v[8:11]\n" \
    "ds_read_b128 v[96:99], %[am1] offset:0\n" \
    "v_mfma_f32_16x16x32_bf16 v[12:15], v[92:95], v[64:67], v[12:15]\n" \
    "v_mfma_f32_16x16x32_bf16 v[16:19], v[80:83], v[68:71], v[16:19]\n" \
    "ds_read_b128 v[116:119], %[an1] offset:2048\n" \
    "v_mfma_f32_16x16x32_bf16 v[20:23], v[84:87], v[68:71], v[20:23]\n" \
    "ds_read_b128 v[100:103], %[am1] offset:2048\n" \
    "v_mfma_f32_16x16x32_bf16 v[24:27], v[88:91], v[68:71], v[24:27]\n" \
    "v_mfma_f32_16x16x32_bf16 v[28:31], v[92:95], v[68:71], v[28:31]\n" \
    "ds_read_b128 v[120:123], %[an1] offset:4096\n" \
    "v_mfma_f32_16x16x32_bf16 v[32:35], v[80:83], v[72:75], v[32:35]\n" \
    "ds_read_b128 v[104:107], %[am1] offset:4096\n" \
    "v_mfma_f32_16x16x32_bf16 v[36:39], v[84:87], v[72:75], v[36:39]\n" \
    "v_mfma_f32_16x16x32_bf16 v[40:43], v[88:91], v[72:75], v[40:43]\n" \
    "ds_read_b128 v[124:127], %[an1] offset:6144\n" \
    "v_mfma_f32_16x16x32_bf16 v[44:47], v[92:95], v[72:75], v[44:47]\n" \
    "ds_read_b128 v[108:111], %[am1] offset:6144\n" \
    "v_mfma_f32_16x16x32_bf16 v[48:51], v[80:83], v[76:79], v[48:51]\n" \
    "v_mfma_f32_16x16x32_bf16 v[52:55], v[84:87], v[76:79], v[52:55]\n" \
    "s_add_u32 %[slot], %[slot], 1\n" \
    "s_and_b32 %[slot], %[slot], 3\n" \
    "s_cmp_eq_u32 %[slot], 0\n" \
    "s_cselect_b32 %[dl], %[dwrap], %[dstep]\n" \
    "v_mfma_f32_16x16x32_bf16 v[56:59], v[88:91], v[76:79], v[56:59]\n" \
    "v_add_u32 %[am0], %[dl], %[am0]\n" \
    "v_add_u32 %[an0], %[dl], %[an0]\n" \
    "v_mfma_f32_16x16x32_bf16 v[60:63], v[92:95], v[76:79], v[60:63]\n" \
    "MID%=:\n" \
    "s_waitcnt lgkmcnt(0)\n" \
    "s_barrier\n" \
    "v_mfma_f32_16x16x32_bf16 v[0:3], v[112:115], v[96:99], v[0:3]\n" \
    "v_mfma_f32_16x16x32_bf16 v[4:7], v[116:119], v[96:99], v[4:7]\n" \
    "ds_read_b128 v[80:83], %[an0] offset:0\n" \
    "v_mfma_f32_16x16x32_bf16 v[8:11], v[120:123], v[96:99], v[8:11]\n" \
    "ds_read_b128 v[64:67], %[am0] offset:0\n" \
    "v_mfma_f32_16x16x32_bf16 v[12:15], v[124:127], v[96:99], v[12:15]\n" \
    "v_mfma_f32_16x16x32_bf16 v[16:19], v[112:115], v[100:103], v[16:19]\n" \
    "ds_read_b128 v[84:87], %[an0] offset:2048\n" \
    "v_mfma_f32_16x16x32_bf16 v[20:23], v[116:119], v[100:103], v[20:23]\n" \
    "ds_read_b128 v[68:71], %[am0] offset:2048\n" \
    "v_mfma_f32_16x16x32_bf16 v[24:27], v[120:123], v[100:103], v[24:27]\n" \
    "v_mfma_f32_16x16x32_bf16 v[28:31], v[124:127], v[100:103], v[28:31]\n" \
    "ds_read_b128 v[88:91], %[an0] offset:4096\n" \
    "v_mfma_f32_16x16x32_bf16 v[32:35], v[112:115], v[104:107], v[32:35]\n" \
    "ds_read_b128 v[72:75], %[am0] offset:4096\n" \
    "v_mfma_f32_16x16x32_bf16 v[36:39], v[116:119], v[104:107], v[36:39]\n" \
    "v_mfma_f32_16x16x32_bf16 v[40:43], v[120:123], v[104:107], v[40:43]\n" \
    "ds_read_b128 v[92:95], %[an0] offset:6144\n" \
    "v_mfma_f32_16x16x32_bf16 v[44:47], v[124:127], v[104:107], v[44:47]\n" \
    "ds_read_b128 v[76:79], %[am0] offset:6144\n" \
    "v_mfma_f32_16x16x32_bf16 v[48:51], v[112:115], v[108:111], v[48:51]\n" \
    "v_mfma_f32_16x16x32_bf16 v[52:55], v[116:119], v[108:111], v[52:55]\n" \
    "v_mfma_f32_16x16x32_bf16 v[56:59], v[120:123], v[108:111], v[56:59]\n" \
    "v_add_u32 %[am1], %[dl], %[am1]\n" \
    "v_add_u32 %[an1], %[dl], %[an1]\n" \
    "s_sub_u32 %[n], %[n], 1\n" \
    "v_mfma_f32_16x16x32_bf16 v[60:63], v[124:127], v[108:111], v[60:63]\n" \
    "s_cmp_lg_u32 %[n], 0\n" \
    "s_cbranch_scc1 TOP%=\n" \
    "s_waitcnt lgkmcnt(0)\n" \
    "s_nop 15\n" \
    ""

#define ASM_T64_TR \
    "s_cmp_eq_u32 %[z], 0\n" \
    "s_cbranch_scc1 TOP%=\n" \
    "s_waitcnt lgkmcnt(0)\n" \
    "v_mfma_f32_16x16x32_bf16 v[0:3], v[80:83], v[64:67], 0\n" \
    "v_mfma_f32_16x16x32_bf16 v[4:7], v[84:87], v[64:67], 0\n" \
    "ds_read_b64_tr_b16 v[112:113], %[an0] offset:8192\n" \
    "v_mfma_f32_16x16x32_bf16 v[8:11], v[88:91], v[64:67], 0\n" \
    "ds_read_b64_tr_b16 v[114:115], %[an0] offset:9216\n" \
    "v_mfma_f32_16x16x32_bf16 v[12:15], v[92:95], v[64:67], 0\n" \
    "ds_read_b128 v[96:99], %[am1] offset:0\n" \
    "v_mfma_f32_16x16x32_bf16 v[16:19], v[80:83], v[68:71], 0\n" \
    "ds_read_b64_tr_b16 v[116:117], %[an1] offset:8192\n" \
    "v_mfma_f32_16x16x32_bf16 v[20:23], v[84:87], v[68:71], 0\n" \
    "ds_read_b64_tr_b16 v[118:119], %[an1] offset:9216\n" \
    "v_mfma_f32_16x16x32_bf16 v[24:27], v[88:91], v[68:71], 0\n" \
    "ds_read_b128 v[100:103], %[am1] offset:2048\n" \
    "v_mfma_f32_16x16x32_bf16 v[28:31], v[92:95], v[68:71], 0\n" \
    "ds_read_b64_tr_b16 v[120:121], %[an2] offset:8192\n" \
    "v_mfma_f32_16x16x32_bf16 v[32:35], v[80:83], v[72:75], 0\n" \
    "ds_read_b64_tr_b16 v[122:123], %[an2] offset:9216\n" \
    "v_mfma_f32_16x16x32_bf16 v[36:39], v[84:87], v[72:75], 0\n" \
    "ds_read_b128 v[104:107], %[am1] offset:4096\n" \
    "v_mfma_f32_16x16x32_bf16 v[40:43], v[88:91], v[72:75], 0\n" \
    "ds_read_b64_tr_b16 v[124:125], %[an3] offset:8192\n" \
    "v_mfma_f32_16x16x32_bf16 v[44:47], v[92:95], v[72:75], 0\n" \
    "ds_read_b64_tr_b16 v[126:127], %[an3] offset:9216\n" \
    "v_mfma_f32_16x16x32_bf16 v[48:51], v[80:83], v[76:79], 0\n" \
    "ds_read_b128 v[108:111], %[am1] offset:6144\n" \
    "v_mfma_f32_16x16x32_bf16 v[52:55], v[84:87], v[76:79], 0\n" \
    "s_add_u32 %[slot], %[slot], 1\n" \
    "s_and_b32 %[slot], %[slot], 3\n" \
    "s_cmp_eq_u32 %[slot], 0\n" \
    "s_cselect_b32 %[dl], %[dwrap], %[dstep]\n" \
    "v_mfma_f32_16x16x32_bf16 v[56:59], v[88:91], v[76:79], 0\n" \
    "v_add_u32 %[am0], %[dl], %[am0]\n" \
    "v_mfma_f32_16x16x32_bf16 v[60:63], v[92:95], v[76:79], 0\n" \
    "v_add_u32 %[an0], %[dl], %[an0]\n" \
    "v_add_u32 %[an1], %[dl], %[an1]\n" \
    "v_add_u32 %[an2], %[dl], %[an2]\n" \
    "v_add_u32 %[an3], %[dl], %[an3]\n" \
    "s_branch MID%=\n" \
    "TOP%=:\n" \
    "s_waitcnt lgkmcnt(0)\n" \
    "v_mfma_f32_16x16x32_bf16 v[0:3], v[80:83], v[64:67], v[0:3]\n" \
    "v_mfma_f32_16x16x32_bf16 v[4:7], v[84:87], v[64:67], v[4:7]\n" \
    "ds_read_b64_tr_b16 v[112:113], %[an0] offset:8192\n" \
    "v_mfma_f32_16x16x32_bf16 v[8:11], v[88:91], v[64:67], v[8:11]\n" \
    "ds_read_b64_tr_b16 v[114:115], %[an0] offset:9216\n" \
    "v_mfma_f32_16x16x32_bf16 v[12:15], v[92:95], v[64:67], v[12:15]\n" \
    "ds_read_b128 v[96:99], %[am1] offset:0\n" \
    "v_mfma_f32_16x16x32_bf16 v[16:19], v[80:83], v[68:71], v[16:19]\n" \
    "ds_read_b64_tr_b16 v[116:117], %[an1] offset:8192\n" \
    "v_mfma_f32_16x16x32_bf16 v[20:23], v[84:87], v[68:71], v[20:23]\n" \
    "ds_read_b64_tr_b16 v[118:119], %[an1] offset:9216\n" \
    "v_mfma_f32_16x16x32_bf16 v[24:27], v[88:91], v[68:71], v[24:27]\n" \
    "ds_read_b128 v[100:103], %[am1] offset:2048\n" \
    "v_mfma_f32_16x16x32_bf16 v[28:31], v[92:95], v[68:71], v[28:31]\n" \
    "ds_read_b64_tr_b16 v[120:121], %[an2] offset:8192\n" \
    "v_mfma_f32_16x16x32_bf16 v[32:35], v[80:83], v[72:75], v[32:35]\n" \
    "ds_read_b64_tr_b16 v[122:123], %[an2] offset:9216\n" \
    "v_mfma_f32_16x16x32_bf16 v[36:39], v[84:87], v[72:75], v[36:39]\n" \
    "ds_read_b128 v[104:107], %[am1] offset:4096\n" \
    "v_mfma_f32_16x16x32_bf16 v[40:43], v[88:91], v[72:75], v[40:43]\n" \
    "ds_read_b64_tr_b16 v[124:125], %[an3] offset:8192\n" \
    "v_mfma_f32_16x16x32_bf16 v[44:47], v[92:95], v[72:75], v[44:47]\n" \
    "ds_read_b64_tr_b16 v[126:127], %[an3] offset:9216\n" \
    "v_mfma_f32_16x16x32_bf16 v[48:51], v[80:83], v[76:79], v[48:51]\n" \
    "ds_read_b128 v[108:111], %[am1] offset:6144\n" \
    "v_mfma_f32_16x16x32_bf16 v[52:55], v[84:87], v[76:79], v[52:55]\n" \
    "s_add_u32 %[slot], %[slot], 1\n" \
    "s_and_b32 %[slot], %[slot], 3\n" \
    "s_cmp_eq_u32 %[slot], 0\n" \
    "s_cselect_b32 %[dl], %[dwrap], %[dstep]\n" \
    "v_mfma_f32_16x16x32_bf16 v[56:59], v[88:91], v[76:79], v[56:59]\n" \
    "v_add_u32 %[am0], %[dl], %[am0]\n" \
    "v_mfma_f32_16x16x32_bf16 v[60:63], v[92:95], v[76:79], v[60:63]\n" \
    "v_add_u32 %[an0], %[dl], %[an0]\n" \
    "v_add_u32 %[an1], %[dl], %[an1]\n" \
    "v_add_u32 %[an2], %[dl], %[an2]\n" \
    "v_add_u32 %[an3], %[dl], %[an3]\n" \
    "MID%=:\n" \
    "s_waitcnt lgkmcnt(0)\n" \
    "s_barrier\n" \
    "v_mfma_f32_16x16x32_bf16 v[0:3], v[112:115], v[96:99], v[0:3]\n" \
    "v_mfma_f32_16x16x32_bf16 v[4:7], v[116:119], v[96:99], v[4:7]\n" \
    "ds_read_b64_tr_b16 v[80:81], %[an0] offset:0\n" \
    "v_mfma_f32_16x16x32_bf16 v[8:11], v[120:123], v[96:99], v[8:11]\n" \
    "ds_read_b64_tr_b16 v[82:83], %[an0] offset:1024\n" \
    "v_mfma_f32_16x16x32_bf16 v[12:15], v[124:127], v[96:99], v[12:15]\n" \
    "ds_read_b128 v[64:67], %[am0] offset:0\n" \
    "v_mfma_f32_16x16x32_bf16 v[16:19], v[112:115], v[100:103], v[16:19]\n" \
    "ds_read_b64_tr_b16 v[84:85], %[an1] offset:0\n" \
    "v_mfma_f32_16x16x32_bf16 v[20:23], v[116:119], v[100:103], v[20:23]\n" \
    "ds_read_b64_tr_b16 v[86:87], %[an1] offset:1024\n" \
    "v_mfma_f32_16x16x32_bf16 v[24:27], v[120:123], v[100:103], v[24:27]\n" \
    "ds_read_b128 v[68:71], %[am0] offset:2048\n" \
    "v_mfma_f32_16x16x32_bf16 v[28:31], v[124:127], v[100:103], v[28:31]\n" \
    "ds_read_b64_tr_b16 v[88:89], %[an2] offset:0\n" \
    "v_mfma_f32_16x16x32_bf16 v[32:35], v[112:115], v[104:107], v[32:35]\n" \
    "ds_read_b64_tr_b16 v[90:91], %[an2] offset:1024\n" \
    "v_mfma_f32_16x16x32_bf16 v[36:39], v[116:119], v[104:107], v[36:39]\n" \
    "ds_read_b128 v[72:75], %[am0] offset:4096\n" \
    "v_mfma_f32_16x16x32_bf16 v[40:43], v[120:123], v[104:107], v[40:43]\n" \
    "ds_read_b64_tr_b16 v[92:93], %[an3] offset:0\n" \
    "v_mfma_f32_16x16x32_bf16 v[44:47], v[124:127], v[104:107], v[44:47]\n" \
    "ds_read_b64_tr_b16 v[94:95], %[an3] offset:1024\n" \
    "v_mfma_f32_16x16x32_bf16 v[48:51], v[112:115], v[108:111], v[48:51]\n" \
    "ds_read_b128 v[76:79], %[am0] offset:6144\n" \
    "v_mfma_f32_16x16x32_bf16 v[52:55], v[116:119], v[108:111], v[52:55]\n" \
    "v_mfma_f32_16x16x32_bf16 v[56:59], v[120:123], v[108:111], v[56:59]\n" \
    "v_add_u32 %[am1], %[dl], %[am1]\n" \
    "s_sub_u32 %[n], %[n], 1\n" \
    "v_mfma_f32_16x16x32_bf16 v[60:63], v[124:127], v[108:111], v[60:63]\n" \
    "s_cmp_lg_u32 %[n], 0\n" \
    "s_cbranch_scc1 TOP%=\n" \
    "s_waitcnt lgkmcnt(0)\n" \
    "s_nop 15\n" \
    ""

#define ASM_QKV \
    "ds_read_b64_tr_b16 v[128:129], %[an0] offset:0\n" \
    "ds_read_b64_tr_b16 v[130:131], %[an0] offset:512\n" \
    "ds_read_b64_tr_b16 v[132:133], %[an1] offset:0\n" \
    "ds_read_b64_tr_b16 v[134:135], %[an1] offset:512\n" \
    "ds_read_b64_tr_b16 v[136:137], %[an0] offset:8192\n" \
    "ds_read_b64_tr_b16 v[138:139], %[an0] offset:8704\n" \
    "ds_read_b128 v[96:99], %[am0] offset:0\n" \
    "ds_read_b128 v[100:103], %[am0] offset:2048\n" \
    "ds_read_b64_tr_b16 v[140:141], %[an1] offset:8192\n" \
    "ds_read_b64_tr_b16 v[142:143], %[an1] offset:8704\n" \
    "ds_read_b128 v[104:107], %[am0] offset:4096\n" \
    "ds_read_b128 v[108:111], %[am0] offset:6144\n" \
    "ds_read_b64_tr_b16 v[144:145], %[an0] offset:16384\n" \
    "ds_read_b64_tr_b16 v[146:147], %[an0] offset:16896\n" \
    "s_waitcnt lgkmcnt(2)\n" \
    "v_mfma_f32_16x16x32_bf16 v[0:3], v[128:131], v[96:99], 0\n" \
    "ds_read_b64_tr_b16 v[148:149], %[an1] offset:16384\n" \
    "v_mfma_f32_16x16x32_bf16 v[24:27], v[128:131], v[100:103], 0\n" \
    "ds_read_b64_tr_b16 v[150:151], %[an1] offset:16896\n" \
    "v_mfma_f32_16x16x32_bf16 v[48:51], v[128:131], v[104:107], 0\n" \
    "v_mfma_f32_16x16x32_bf16 v[72:75], v[128:131], v[108:111], 0\n" \
    "v_mfma_f32_16x16x32_bf16 v[4:7], v[132:135], v[96:99], 0\n" \
    "ds_read_b64_tr_b16 v[128:129], %[an0] offset:4096\n" \
    "v_mfma_f32_16x16x32_bf16 v[28:31], v[132:135], v[100:103], 0\n" \
    "ds_read_b64_tr_b16 v[130:131], %[an0] offset:4608\n" \
    "v_mfma_f32_16x16x32_bf16 v[52:55], v[132:135], v[104:107], 0\n" \
    "ds_read_b128 v[112:115], %[am1] offset:0\n" \
    "v_mfma_f32_16x16x32_bf16 v[76:79], v[132:135], v[108:111], 0\n" \
    "v_mfma_f32_16x16x32_bf16 v[8:11], v[136:139], v[96:99], 0\n" \
    "ds_read_b64_tr_b16 v[132:133], %[an1] offset:4096\n" \
    "v_mfma_f32_16x16x32_bf16 v[32:35], v[136:139], v[100:103], 0\n" \
    "ds_read_b64_tr_b16 v[134:135], %[an1] offset:4608\n" \
    "v_mfma_f32_16x16x32_bf16 v[56:59], v[136:139], v[104:107], 0\n" \
    "ds_read_b128 v[116:119], %[am1] offset:2048\n" \
    "v_mfma_f32_16x16x32_bf16 v[80:83], v[136:139], v[108:111], 0\n" \
    "v_mfma_f32_16x16x32_bf16 v[12:15], v[140:143], v[96:99], 0\n" \
    "ds_read_b64_tr_b16 v[136:137], %[an0] offset:12288\n" \
    "v_mfma_f32_16x16x32_bf16 v[36:39], v[140:143], v[100:103], 0\n" \
    "ds_read_b64_tr_b16 v[138:139], %[an0] offset:12800\n" \
    "v_mfma_f32_16x16x32_bf16 v[60:63], v[140:143], v[104:107], 0\n" \
    "ds_read_b128 v[120:123], %[am1] offset:4096\n" \
    "v_mfma_f32_16x16x32_bf16 v[84:87], v[140:143], v[108:111], 0\n" \
    "s_waitcnt lgkmcnt(11)\n" \
    "v_mfma_f32_16x16x32_bf16 v[16:19], v[144:147], v[96:99], 0\n" \
    "ds_read_b64_tr_b16 v[140:141], %[an1] offset:12288\n" \
    "v_mfma_f32_16x16x32_bf16 v[40:43], v[144:147], v[100:103], 0\n" \
    "ds_read_b64_tr_b16 v[142:143], %[an1] offset:12800\n" \
    "v_mfma_f32_16x16x32_bf16 v[64:67], v[144:147], v[104:107], 0\n" \
    "ds_read_b128 v[124:127], %[am1] offset:6144\n" \
    "v_mfma_f32_16x16x32_bf16 v[88:91], v[144:147], v[108:111], 0\n" \
    "s_waitcnt lgkmcnt(12)\n" \
    "v_mfma_f32_16x16x32_bf16 v[20:23], v[148:151], v[96:99], 0\n" \
    "ds_read_b64_tr_b16 v[144:145], %[an0] offset:20480\n" \
    "v_mfma_f32_16x16x32_bf16 v[44:47], v[148:151], v[100:103], 0\n" \
    "ds_read_b64_tr_b16 v[146:147], %[an0] offset:20992\n" \
    "v_mfma_f32_16x16x32_bf16 v[68:71], v[148:151], v[104:107], 0\n" \
    "s_add_u32 %[ms], %[ms], 1\n" \
    "s_cmp_eq_u32 %[ms], 3\n" \
    "s_cselect_b32 %[ms], 0, %[ms]\n" \
    "s_cmp_eq_u32 %[ms], 0\n" \
    "s_cselect_b32 %[dm], %[mwrap], %[mstep]\n" \
    "v_mfma_f32_16x16x32_bf16 v[92:95], v[148:151], v[108:111], 0\n" \
    "s_xor_b32 %[ns], %[ns], 1\n" \
    "s_cmp_eq_u32 %[ns], 0\n" \
    "s_cselect_b32 %[dn], %[nwrap], %[nstep]\n" \
    "s_branch MID%=\n" \
    "TOP%=:\n" \
    "s_waitcnt lgkmcnt(2)\n" \
    "v_mfma_f32_16x16x32_bf16 v[0:3], v[128:131], v[96:99], v[0:3]\n" \
    "ds_read_b64_tr_b16 v[148:149], %[an1] offset:16384\n" \
    "v_mfma_f32_16x16x32_bf16 v[24:27], v[128:131], v[100:103], v[24:27]\n" \
    "ds_read_b64_tr_b16 v[150:151], %[an1] offset:16896\n" \
    "v_mfma_f32_16x16x32_bf16 v[48:51], v[128:131], v[104:107], v[48:51]\n" \
    "v_mfma_f32_16x16x32_bf16 v[72:75], v[128:131], v[108:111], v[72:75]\n" \
    "v_mfma_f32_16x16x32_bf16 v[4:7], v[132:135], v[96:99], v[4:7]\n" \
    "ds_read_b64_tr_b16 v[128:129], %[an0] offset:4096\n" \
    "v_mfma_f32_16x16x32_bf16 v[28:31], v[132:135], v[100:103], v[28:31]\n" \
    "ds_read_b64_tr_b16 v[130:131], %[an0] offset:4608\n" \
    "v_mfma_f32_16x16x32_bf16 v[52:55], v[132:135], v[104:107], v[52:55]\n" \
    "ds_read_b128 v[112:115], %[am1] offset:0\n" \
    "v_mfma_f32_16x16x32_bf16 v[76:79], v[132:135], v[108:111], v[76:79]\n" \
    "v_mfma_f32_16x16x32_bf16 v[8:11], v[136:139], v[96:99], v[8:11]\n" \
    "ds_read_b64_tr_b16 v[132:133], %[an1] offset:4096\n" \
    "v_mfma_f32_16x16x32_bf16 v[32:35], v[136:139], v[100:103], v[32:35]\n" \
    "ds_read_b64_tr_b16 v[134:135], %[an1] offset:4608\n" \
    "v_mfma_f32_16x16x32_bf16 v[56:59], v[136:139], v[104:107], v[56:59]\n" \
    "ds_read_b128 v[116:119], %[am1] offset:2048\n" \
    "v_mfma_f32_16x16x32_bf16 v[80:83], v[136:139], v[108:111], v[80:83]\n" \
    "v_mfma_f32_16x16x32_bf16 v[12:15], v[140:143], v[96:99], v[12:15]\n" \
    "ds_read_b64_tr_b16 v[136:137], %[an0] offset:12288\n" \
    "v_mfma_f32_16x16x32_bf16 v[36:39], v[140:143], v[100:103], v[36:39]\n" \
    "ds_read_b64_tr_b16 v[138:139], %[an0] offset:12800\n" \
    "v_mfma_f32_16x16x32_bf16 v[60:63], v[140:143], v[104:107], v[60:63]\n" \
    "ds_read_b128 v[120:123], %[am1] offset:4096\n" \
    "v_mfma_f32_16x16x32_bf16 v[84:87], v[140:143], v[108:111], v[84:87]\n" \
    "s_waitcnt lgkmcnt(11)\n" \
    "v_mfma_f32_16x16x32_bf16 v[16:19], v[144:147], v[96:99], v[16:19]\n" \
    "ds_read_b64_tr_b16 v[140:141], %[an1] offset:12288\n" \
    "v_mfma_f32_16x16x32_bf16 v[40:43], v[144:147], v[100:103], v[40:43]\n" \
    "ds_read_b64_tr_b16 v[142:143], %[an1] offset:12800\n" \
    "v_mfma_f32_16x16x32_bf16 v[64:67], v[144:147], v[104:107], v[64:67]\n" \
    "ds_read_b128 v[124:127], %[am1] offset:6144\n" \
    "v_mfma_f32_16x16x32_bf16 v[88:91], v[144:147], v[108:111], v[88:91]\n" \
    "s_waitcnt lgkmcnt(12)\n" \
    "v_mfma_f32_16x16x32_bf16 v[20:23], v[148:151], v[96:99], v[20:23]\n" \
    "ds_read_b64_tr_b16 v[144:145], %[an0] offset:20480\n" \
    "v_mfma_f32_16x16x32_bf16 v[44:47], v[148:151], v[100:103], v[44:47]\n" \
    "ds_read_b64_tr_b16 v[146:147], %[an0] offset:20992\n" \
    "v_mfma_f32_16x16x32_bf16 v[68:71], v[148:151], v[104:107], v[68:71]\n" \
    "s_add_u32 %[ms], %[ms], 1\n" \
    "s_cmp_eq_u32 %[ms], 3\n" \
    "s_cselect_b32 %[ms], 0, %[ms]\n" \
    "s_cmp_eq_u32 %[ms], 0\n" \
    "s_cselect_b32 %[dm], %[mwrap], %[mstep]\n" \
    "v_mfma_f32_16x16x32_bf16 v[92:95], v[148:151], v[108:111], v[92:95]\n" \
    "s_xor_b32 %[ns], %[ns], 1\n" \
    "s_cmp_eq_u32 %[ns], 0\n" \
    "s_cselect_b32 %[dn], %[nwrap], %[nstep]\n" \
    "MID%=:\n" \
    "s_waitcnt lgkmcnt(2)\n" \
    "v_mfma_f32_16x16x32_bf16 v[0:3], v[128:131], v[112:115], v[0:3]\n" \
    "ds_read_b64_tr_b16 v[148:149], %[an1] offset:20480\n" \
    "v_mfma_f32_16x16x32_bf16 v[24:27], v[128:131], v[116:119], v[24:27]\n" \
    "ds_read_b64_tr_b16 v[150:151], %[an1] offset:20992\n" \
    "v_mfma_f32_16x16x32_bf16 v[48:51], v[128:131], v[120:123], v[48:51]\n" \
    "v_add_u32 %[am0], %[dm], %[am0]\n" \
    "v_mfma_f32_16x16x32_bf16 v[72:75], v[128:131], v[124:127], v[72:75]\n" \
    "v_add_u32 %[am1], %[dm], %[am1]\n" \
    "v_mfma_f32_16x16x32_bf16 v[4:7], v[132:135], v[112:115], v[4:7]\n" \
    "v_add_u32 %[an0], %[dn], %[an0]\n" \
    "v_mfma_f32_16x16x32_bf16 v[28:31], v[132:135], v[116:119], v[28:31]\n" \
    "v_add_u32 %[an1], %[dn], %[an1]\n" \
    "v_mfma_f32_16x16x32_bf16 v[52:55], v[132:135], v[120:123], v[52:55]\n" \
    "v_mfma_f32_16x16x32_bf16 v[76:79], v[132:135], v[124:127], v[76:79]\n" \
    "s_waitcnt lgkmcnt(0)\n" \
    "s_barrier\n" \
    "v_mfma_f32_16x16x32_bf16 v[8:11], v[136:139], v[112:115], v[8:11]\n" \
    "ds_read_b64_tr_b16 v[128:129], %[an0] offset:0\n" \
    "v_mfma_f32_16x16x32_bf16 v[32:35], v[136:139], v[116:119], v[32:35]\n" \
    "ds_read_b64_tr_b16 v[130:131], %[an0] offset:512\n" \
    "v_mfma_f32_16x16x32_bf16 v[56:59], v[136:139], v[120:123], v[56:59]\n" \
    "ds_read_b64_tr_b16 v[132:133], %[an1] offset:0\n" \
    "v_mfma_f32_16x16x32_bf16 v[80:83], v[136:139], v[124:127], v[80:83]\n" \
    "ds_read_b64_tr_b16 v[134:135], %[an1] offset:512\n" \
    "v_mfma_f32_16x16x32_bf16 v[12:15], v[140:143], v[112:115], v[12:15]\n" \
    "ds_read_b64_tr_b16 v[136:137], %[an0] offset:8192\n" \
    "v_mfma_f32_16x16x32_bf16 v[36:39], v[140:143], v[116:119], v[36:39]\n" \
    "ds_read_b64_tr_b16 v[138:139], %[an0] offset:8704\n" \
    "v_mfma_f32_16x16x32_bf16 v[60:63], v[140:143], v[120:123], v[60:63]\n" \
    "ds_read_b128 v[96:99], %[am0] offset:0\n" \
    "v_mfma_f32_16x16x32_bf16 v[84:87], v[140:143], v[124:127], v[84:87]\n" \
    "ds_read_b128 v[100:103], %[am0] offset:2048\n" \
    "v_mfma_f32_16x16x32_bf16 v[16:19], v[144:147], v[112:115], v[16:19]\n" \
    "ds_read_b64_tr_b16 v[140:141], %[an1] offset:8192\n" \
    "v_mfma_f32_16x16x32_bf16 v[40:43], v[144:147], v[116:119], v[40:43]\n" \
    "ds_read_b64_tr_b16 v[142:143], %[an1] offset:8704\n" \
    "v_mfma_f32_16x16x32_bf16 v[64:67], v[144:147], v[120:123], v[64:67]\n" \
    "ds_read_b128 v[104:107], %[am0] offset:4096\n" \
    "v_mfma_f32_16x16x32_bf16 v[88:91], v[144:147], v[124:127], v[88:91]\n" \
    "ds_read_b128 v[108:111], %[am0] offset:6144\n" \
    "v_mfma_f32_16x16x32_bf16 v[20:23], v[148:151], v[112:115], v[20:23]\n" \
    "ds_read_b64_tr_b16 v[144:145], %[an0] offset:16384\n" \
    "v_mfma_f32_16x16x32_bf16 v[44:47], v[148:151], v[116:119], v[44:47]\n" \
    "ds_read_b64_tr_b16 v[146:147], %[an0] offset:16896\n" \
    "v_mfma_f32_16x16x32_bf16 v[68:71], v[148:151], v[120:123], v[68:71]\n" \
    "s_sub_u32 %[n], %[n], 1\n" \
    "v_mfma_f32_16x16x32_bf16 v[92:95], v[148:151], v[124:127], v[92:95]\n" \
    "s_cmp_lg_u32 %[n], 0\n" \
    "s_cbranch_scc1 TOP%=\n" \
    "s_waitcnt lgkmcnt(0)\n" \
    "s_nop 15\n" \
    ""

static constexpr size_t WS_ABT = 0;
static constexpr size_t WS_O = 8u << 20;
static constexpr size_t WS_KV = 12u << 20;
static constexpr size_t WS_CTL = 24u << 20;
static constexpr int CTL_EPOCH = 0;
static constexpr int CTL_ARR = 1;
static constexpr int CTL_FA = CTL_ARR + 256;
static constexpr int CTL_FB = CTL_FA + 128;
static constexpr int CTL_KV = CTL_FB + 128;
static constexpr int CTL_FO = CTL_KV + 128;

static constexpr int LDS_BYTES = 160 * 1024;
static constexpr int LDS_MAIL = 128 * 1024;
#define DRAIN_STEPS 6
#define PF_DIST 0
static constexpr int SPIN_LIMIT = 1 << 17;

__device__ __forceinline__ void wg_barrier() { asm volatile("s_barrier" ::: "memory"); }
__device__ __forceinline__ int uni(int v) { return __builtin_amdgcn_readfirstlane(v); }
__device__ __forceinline__ int fresh(int v) { asm volatile("" : "+v"(v)); return v; }

__device__ __forceinline__ i32x4 make_srd(const void* p, unsigned bytes) {
    i32x4 r;
    r[0] = uni((int)(unsigned)(size_t)p);
    r[1] = uni((int)(unsigned)((size_t)p >> 32));
    r[2] = (int)bytes;
    r[3] = 0x00020000;
    return r;
}
__device__ __forceinline__ void dma16(unsigned voff, i32x4 srd, unsigned soff, unsigned lds) {
    asm volatile("s_mov_b32 m0, %3\n\ts_nop 0\n\tbuffer_load_dwordx4 %0, %1, %2 offen lds"
                 :: "v"(voff), "s"(srd), "s"(soff), "s"(lds) : "memory");
}
__device__ __forceinline__ void dma16_dev(unsigned voff, i32x4 srd, unsigned soff, unsigned lds) {
    asm volatile("s_mov_b32 m0, %3\n\ts_nop 0\n\tbuffer_load_dwordx4 %0, %1, %2 offen sc1 lds"
                 :: "v"(voff), "s"(srd), "s"(soff), "s"(lds) : "memory");
}
template <int N> __device__ __forceinline__ void wait_vm() { asm volatile("s_waitcnt vmcnt(%0)" :: "n"(N) : "memory"); }
__device__ __forceinline__ void wait_lgkm0() { asm volatile("s_waitcnt lgkmcnt(0)" ::: "memory"); }
__device__ __forceinline__ void acquire_agent() { asm volatile("buffer_inv sc1" ::: "memory"); }
__device__ __forceinline__ unsigned pack2(float a, float b) {
    bf16x2 v = {(__bf16)a, (__bf16)b};
    return __builtin_bit_cast(unsigned, v);
}
__device__ __forceinline__ void store16_dev(char* base, unsigned voff, u32x4 v) {
    asm volatile("global_store_dwordx4 %0, %1, %2 sc1\n\ts_nop 1" :: "v"(voff), "v"(v), "s"(base) : "memory");
}
__device__ __forceinline__ void store16(char* base, unsigned voff, u32x4 v) {
    asm volatile("global_store_dwordx4 %0, %1, %2\n\ts_nop 1" :: "v"(voff), "v"(v), "s"(base) : "memory");
}
__device__ __forceinline__ GAS u64* ctl_line(char* ws, int line) { return (GAS u64*)(ws + WS_CTL + (size_t)line * 128); }
__device__ __forceinline__ void flag_store(char* ws, int line, u64 tag) {
    __hip_atomic_store(ctl_line(ws, line), tag, __ATOMIC_RELAXED, __HIP_MEMORY_SCOPE_AGENT);
}
__device__ __forceinline__ u64 flag_load(char* ws, int line) {
    return __hip_atomic_load(ctl_line(ws, line), __ATOMIC_RELAXED, __HIP_MEMORY_SCOPE_AGENT);
}

struct T64State { f32x32 acc0, acc1; i32x16 fa0, fa1; };

__device__ __forceinline__ void t64_nt(f32x32& acc0, f32x32& acc1, i32x16& fm, i32x16& fn, unsigned& am0, unsigned& am1,
                                       unsigned& an0, unsigned& an1, int& slot, int n, int zero) {
    int dl;
    asm volatile(ASM_T64_NT
                 : "+{v[0:31]}"(acc0), "+{v[32:63]}"(acc1), "+{v[64:79]}"(fm), "+{v[80:95]}"(fn),
                   [am0] "+v"(am0), [am1] "+v"(am1), [an0] "+v"(an0), [an1] "+v"(an1),
                   [slot] "+s"(slot), [n] "+s"(n), [dl] "=&s"(dl)
                 : [z] "s"(zero), [dwrap] "s"(-98304), [dstep] "s"(32768)
                 : "v96", "v97", "v98", "v99", "v100", "v101", "v102", "v103", "v104", "v105", "v106", "v107",
                   "v108", "v109", "v110", "v111", "v112", "v113", "v114", "v115", "v116", "v117", "v118", "v119",
                   "v120", "v121", "v122", "v123", "v124", "v125", "v126", "v127", "scc", "memory");
}

template <bool DEV>
__device__ __forceinline__ void t64_store_perm(const f32x32& acc0, const f32x32& acc1, char* base, unsigned voff, unsigned rstride16,
                                               unsigned sign) {
#pragma unroll
    for (int i = 0; i < 4; ++i) {
#pragma unroll
        for (int J = 0; J < 2; ++J) {
            const int r = (i & 1) * 16 + J * 8;
            const f32x32& a = (i < 2) ? acc0 : acc1;
            u32x4 v;
            v[0] = pack2(a[r + 0], a[r + 1]) ^ sign;
            v[1] = pack2(a[r + 2], a[r + 3]) ^ sign;
            v[2] = pack2(a[r + 4], a[r + 5]) ^ sign;
            v[3] = pack2(a[r + 6], a[r + 7]) ^ sign;
            if (DEV) store16_dev(base, voff + i * rstride16 + J * 64, v);
            else store16(base, voff + i * rstride16 + J * 64, v);
        }
    }
}

__device__ __forceinline__ void spectral_consumer(int cw, int lane, char* ws, char* out, int eb, int sb, int tb, int eb3) {
    const int wm = cw >> 1, wn = cw & 1;
    const unsigned pos = (unsigned)(((lane >> 4) ^ ((lane & 15) >> 1)) * 16);
    unsigned am0 = (unsigned)((64 * wm + (lane & 15)) * 128) + pos;
    unsigned an0 = 16384u + (unsigned)((64 * wn + (lane & 15)) * 128) + pos;
    unsigned am1 = am0 ^ 64u, an1 = an0 ^ 64u;
    f32x32 acc0, acc1;
    i32x16 fm, fn;
    wg_barrier();
    asm volatile(
        "ds_read_b128 v[64:67], %2\n ds_read_b128 v[68:71], %2 offset:2048\n ds_read_b128 v[72:75], %2 offset:4096\n ds_read_b128 v[76:79], %2 offset:6144\n"
        "ds_read_b128 v[80:83], %3\n ds_read_b128 v[84:87], %3 offset:2048\n ds_read_b128 v[88:91], %3 offset:4096\n ds_read_b128 v[92:95], %3 offset:6144\n"
        "s_waitcnt lgkmcnt(0)\n"
        : "={v[64:79]}"(fm), "={v[80:95]}"(fn) : "v"(am0), "v"(an0) : "memory");
    asm volatile("" : "={v[0:31]}"(acc0), "={v[32:63]}"(acc1));
    int slot = 0;
    const unsigned g8 = (unsigned)(lane >> 4) * 16u;
#pragma unroll 1
    for (int seg = 0; seg < 5; ++seg) {
        constexpr u64 NSEG = 16ull | ((u64)DRAIN_STEPS << 8) | ((u64)(16 - DRAIN_STEPS) << 16) | ((u64)DRAIN_STEPS << 24) |
                             ((u64)(64 - DRAIN_STEPS) << 32);
        const int n = (int)((NSEG >> (8 * seg)) & 0xff);
        const int zero = (0xb >> seg) & 1;
        t64_nt(acc0, acc1, fm, fn, am0, am1, an0, an1, slot, n, zero);
        if (seg == 1 || seg == 3) {
            wait_vm<0>();
        } else if (seg != 4) {
            char* base = ws + WS_ABT + (size_t)(eb * 128 + 64 * wm) * 8192 + (size_t)(sb * 128 + 64 * wn) * 2 + (seg == 2 ? 4096 : 0);
            t64_store_perm<true>(acc0, acc1, base, (unsigned)(lane & 15) * 8192u + g8, 16u * 8192u, seg == 2 ? 0x80008000u : 0u);
        } else {
            char* base = out + (size_t)(tb * 128 + 64 * wm) * 4096 + (size_t)(1024 + eb3 * 128 + 64 * wn) * 2;
            t64_store_perm<false>(acc0, acc1, base, (unsigned)(lane & 15) * 4096u + g8, 16u * 4096u, 0u);
        }
    }
}

#define EXPM 0
#define EXPQ 0
#define BONLY 0
#define AONLY 0
#define NLOAD 4
#define ROT 0
__device__ __forceinline__ void spectral_loader(int lw, int lane, const char* Bx, const char* Cr, const char* Ci, const char* Dr,
                                                const char* Di, char* ws, int eb, int sb, int tb, int eb3, int x) {
    constexpr int PER = 16 / NLOAD;
    const unsigned swz = (unsigned)(((lane & 7) ^ (4 * (lw & 1) + (lane >> 4))) * 16);
    const unsigned mrow = (unsigned)(8 * lw + (lane >> 3));
    const unsigned nrow = (unsigned)(32 * (lw >> 2) + 8 * (2 * (lw & 1) + (lane >> 5)) + 4 * ((lw >> 1) & 1) + ((lane >> 3) & 3));
    const unsigned ldsw = (unsigned)lw * 1024u;
    int s = 0;
#pragma unroll 1
    for (int ph = 0; ph < 4; ++ph) {
        const unsigned ms = ph < 2 ? 2048u : 4096u, ns = ph < 2 ? 2048u : 8192u;
        const char* mp = (ph == 0 ? Dr : ph == 1 ? Di : ph == 2 ? Cr : Ci) + (size_t)(ph < 2 ? eb : tb) * 128 * ms;
        const char* np = ph < 2 ? Bx + (size_t)sb * 128 * 2048 : ws + WS_ABT + (size_t)eb3 * 128 * 8192 + (ph == 3 ? 4096 : 0);
        const i32x4 srdm = make_srd(mp, 128 * ms), srdn = make_srd(np, 128 * ns);
        const unsigned vm = mrow * ms + swz, vn = nrow * ns + swz;
        const int send = ph == 0 ? 16 : 32 * ph;
        const unsigned kmask = ph < 2 ? 2047u : 4095u;
        unsigned ko = (unsigned)(ROT * x * (ph < 2 ? 256 : 512)) & kmask;
#pragma unroll 1
        for (; s < send; ++s) {
            if (s >= 4) { wait_vm<4 * PER>(); wg_barrier(); }
            const unsigned lds = (unsigned)(s & 3) * 32768u + ldsw;
#pragma unroll
            for (int i = 0; i < PER; ++i) dma16(vm, srdm, ko + i * 8 * NLOAD * ms, lds + i * 1024u * NLOAD);
#pragma unroll
            for (int i = 0; i < PER; ++i) dma16(vn, srdn, ko + i * 8 * NLOAD * ns, lds + 16384u + i * 1024u * NLOAD);
            ko = (ko + 128) & kmask;

            if (s == 3) { wait_vm<6 * PER>(); wg_barrier(); }
        }
    }
    wait_vm<4 * PER>(); wg_barrier();
    wait_vm<2 * PER>(); wg_barrier();
    wait_vm<0>(); wg_barrier();
    wg_barrier();
}

struct Gate {
    int state;
};
__device__ __forceinline__ bool mail_match(unsigned lds, int lane, u64 tag) {
    u32x2 v;
    asm volatile("ds_read_b64 %0, %1\n\ts_waitcnt lgkmcnt(0)" : "=v"(v) : "v"(lds + (unsigned)lane * 16u) : "memory");
    const u64 x = ((u64)v[1] << 32) | v[0];
    return __all(x == tag);
}
__device__ __forceinline__ void mail_clear(unsigned lds, int lane, u64 tag) {
    u32x2 v = {~(unsigned)tag, ~(unsigned)(tag >> 32)};
    asm volatile("ds_write_b64 %0, %1\n\ts_waitcnt lgkmcnt(0)" :: "v"(lds + (unsigned)lane * 16u), "v"(v) : "memory");
}
__device__ __forceinline__ int gate_step(int state, int lane, i32x4 srdc, unsigned voff, u64 tag) {
    if (state == 1) {
        if (mail_match(LDS_MAIL, lane, tag)) {
            acquire_agent();
            dma16_dev(voff, srdc, 0, LDS_MAIL + 1024);
            state = 2;
        } else {
            dma16_dev(voff, srdc, 0, LDS_MAIL);
        }
    } else if (state == 2) {
        if (mail_match(LDS_MAIL + 1024, lane, tag)) { wait_vm<0>(); state = 3; }
    }
    return state;
}

__device__ __forceinline__ void spectral_control(int lane, char* ws, int w, int eb, int sb, int eb3) {
    u64 tag;
    {
        u32x2 e;
        GAS u64* ep = ctl_line(ws, CTL_EPOCH);
        asm volatile("global_load_dwordx2 %0, %1, off sc1" : "=v"(e) : "v"(ep) : "memory");
        wg_barrier();
        wg_barrier();
        wg_barrier();
        asm volatile("s_waitcnt vmcnt(0)" : "+v"(e) :: "memory");
        tag = (((u64)e[1] << 32) | e[0]) + 1;
    }
    if (lane == 0) flag_store(ws, CTL_ARR + w, tag);
    const i32x4 srdc = make_srd(ws + WS_CTL, 1024 * 128);
    int state = 0;
    unsigned voff = 0;
#pragma unroll 1
    for (int k = 2; k < 96; ++k) {
        if (k == 16 + DRAIN_STEPS + 1 && lane == 0) flag_store(ws, CTL_FA + eb * 16 + sb, tag);
        if (k == 32 + DRAIN_STEPS + 1 && lane == 0) flag_store(ws, CTL_FB + eb * 16 + sb, tag);
        if (k == 16 + DRAIN_STEPS + 2 || k == 40) {
            voff = (unsigned)(((k == 40 ? CTL_FB : CTL_FA) + eb3 * 16 + (lane & 15)) * 128);
            mail_clear(LDS_MAIL, lane, tag);
            mail_clear(LDS_MAIL + 1024, lane, tag);
            dma16_dev(voff, srdc, 0, LDS_MAIL);
            state = 1;
        } else if (state == 1 || state == 2) {
            state = gate_step(state, lane, srdc, voff, tag);
        }
        if (k == 28 || k == 60) {
            for (int spin = 0; spin < SPIN_LIMIT && state != 3; ++spin) {
                __builtin_amdgcn_s_sleep(2);
                state = gate_step(state, lane, srdc, voff, tag);
            }
            state = 0;
        }
        if (w == 0 && k == 70) {
            for (int spin = 0; spin < SPIN_LIMIT; ++spin) {
                bool ok = true;
#pragma unroll
                for (int q = 0; q < 4; ++q) ok = ok && (flag_load(ws, CTL_ARR + q * 64 + lane) == tag);
                if (__all(ok)) break;
                __builtin_amdgcn_s_sleep(4);
            }
            if (lane == 0) flag_store(ws, CTL_EPOCH, tag);
        }
        wg_barrier();
    }
}

__device__ __forceinline__ void pf4(unsigned voff, i32x4 srd, unsigned soff, unsigned lds) {
    asm volatile("s_mov_b32 m0, %3\n\ts_nop 0\n\tbuffer_load_dword %0, %1, %2 offen lds"
                 :: "v"(voff), "s"(srd), "s"(soff), "s"(lds) : "memory");
}
__device__ __forceinline__ void spectral_prefetch(int lane, const char* Bx, const char* Cr, const char* Ci, const char* Dr,
                                                  const char* Di, char* ws, int idx, int eb, int sb, int tb, int eb3) {
    const unsigned ja = (unsigned)(idx & 3), jb = (unsigned)(idx >> 2);
    const unsigned r = (unsigned)(lane & 31), half = (unsigned)(lane >> 5) * 128u;
    const unsigned vm_lo = (32 * jb + r) * 2048u + half, vn_lo = (32 * ja + r) * 2048u + half;
    const unsigned vm_hi = (32 * ja + r) * 4096u + half, vn_hi = (32 * jb + r) * 8192u + half;
    const i32x4 sdr = make_srd(Dr + (size_t)eb * 128 * 2048, 128 * 2048), sdi = make_srd(Di + (size_t)eb * 128 * 2048, 128 * 2048);
    const i32x4 scr = make_srd(Cr + (size_t)tb * 128 * 4096, 128 * 4096), sci = make_srd(Ci + (size_t)tb * 128 * 4096, 128 * 4096);
    const i32x4 sx = make_srd(Bx + (size_t)sb * 128 * 2048, 128 * 2048);
    const i32x4 sab = make_srd(ws + WS_ABT + (size_t)eb3 * 128 * 8192, 128 * 8192);
    int nm = 4, nn = 4;
#pragma unroll 1
    for (int k = -1; k < 96; ++k) {
        while (nm <= k + PF_DIST && nm < 96) {
            if (nm < 16) pf4(vm_lo, sdr, nm * 128, LDS_MAIL + 2048);
            else if (nm < 32) pf4(vm_lo, sdi, (nm - 16) * 128, LDS_MAIL + 2048);
            else if (nm < 64) pf4(vm_hi, scr, (nm - 32) * 128, LDS_MAIL + 2048);
            else pf4(vm_hi, sci, (nm - 64) * 128, LDS_MAIL + 2048);
            nm += 2;
        }
        const int lim = k < 28 ? 32 : k < 60 ? 64 : 96;
        while (nn <= k + PF_DIST - 1 && nn < lim) {
            if (nn < 16) pf4(vn_lo, sx, nn * 128, LDS_MAIL + 2048);
            else if (nn >= 32 && nn > k + 4) pf4(vn_hi, sab, (nn - 32) * 128, LDS_MAIL + 2048);
            nn += 2;
        }
        if (k == -1) wg_barrier();
        if (k < 95) wg_barrier();
    }
    wait_vm<0>();
}

static constexpr unsigned LDS_NRING = 96 * 1024;
static constexpr unsigned LDS_PREV = 64 * 1024;
static constexpr unsigned LDS_WORDS = 156 * 1024;
static constexpr unsigned LDS_TAG = LDS_WORDS, LDS_CNT = LDS_WORDS + 8, LDS_RDY_KV = LDS_WORDS + 12, LDS_RDY_O = LDS_WORDS + 16;
static constexpr float Q_SCALE = 0.17677669529663687f * 1.4426950408889634f;

__device__ __forceinline__ f32x4 mfma16(bf16x8 a, bf16x8 b, f32x4 c) { return __builtin_amdgcn_mfma_f32_16x16x32_bf16(a, b, c, 0, 0, 0); }
__device__ __forceinline__ bf16x8 lds_read16(unsigned a) { return *(LAS bf16x8*)(size_t)a; }
__device__ __forceinline__ bf16x4 lds_read_tr(unsigned a) { return __builtin_amdgcn_ds_read_tr16_b64_v4bf16((LAS bf16x4*)(size_t)a); }
__device__ __forceinline__ void lds_write16(unsigned a, u32x4 v) { *(LAS u32x4*)(size_t)a = v; }
__device__ __forceinline__ unsigned lds_word(unsigned a) { return *(volatile LAS unsigned*)(size_t)a; }
__device__ __forceinline__ void lds_set(unsigned a, unsigned v) { *(volatile LAS unsigned*)(size_t)a = v; }
__device__ __forceinline__ u64 lds_tag() {
    const u32x2 v = *(volatile LAS u32x2*)(size_t)LDS_TAG;
    return ((u64)v[1] << 32) | v[0];
}
__device__ __forceinline__ void swap16(unsigned& a, unsigned& b) {
    auto r = __builtin_amdgcn_permlane16_swap(a, b, false, false);
    a = r[0]; b = r[1];
}
__device__ __forceinline__ float row_max(float x) {
    auto r = __builtin_amdgcn_permlane32_swap(__float_as_uint(x), __float_as_uint(x), false, false);
    x = __builtin_fmaxf(__uint_as_float(r[0]), __uint_as_float(r[1]));
    auto t = __builtin_amdgcn_permlane16_swap(__float_as_uint(x), __float_as_uint(x), false, false);
    return __builtin_fmaxf(__uint_as_float(t[0]), __uint_as_float(t[1]));
}
__device__ __forceinline__ float max4(float m, f32x4 s) {
    return __builtin_fmaxf(__builtin_fmaxf(__builtin_fmaxf(__builtin_fmaxf(m, s[0]), s[1]), s[2]), s[3]);
}
__device__ __forceinline__ bf16x8 as_frag(u32x4 v) { return __builtin_bit_cast(bf16x8, v); }

__device__ __forceinline__ void qkv_gemm(f32x32& a0, f32x32& a1, f32x32& a2, unsigned am0, unsigned am1, unsigned an0, unsigned an1) {
    int ms = 0, ns = 0, n = 16, dm, dn;
    asm volatile(ASM_QKV
                 : "={v[0:31]}"(a0), "={v[32:63]}"(a1), "={v[64:95]}"(a2),
                   [am0] "+v"(am0), [am1] "+v"(am1), [an0] "+v"(an0), [an1] "+v"(an1),
                   [ms] "+s"(ms), [ns] "+s"(ns), [n] "+s"(n), [dm] "=&s"(dm), [dn] "=&s"(dn)
                 : [mwrap] "s"(-65536), [mstep] "s"(32768), [nwrap] "s"(-24576), [nstep] "s"(24576)
                 : "v96", "v97", "v98", "v99", "v100", "v101", "v102", "v103", "v104", "v105", "v106", "v107",
                   "v108", "v109", "v110", "v111", "v112", "v113", "v114", "v115", "v116", "v117", "v118", "v119",
                   "v120", "v121", "v122", "v123", "v124", "v125", "v126", "v127", "v128", "v129", "v130", "v131",
                   "v132", "v133", "v134", "v135", "v136", "v137", "v138", "v139", "v140", "v141", "v142", "v143",
                   "v144", "v145", "v146", "v147", "v148", "v149", "v150", "v151", "scc", "memory");
}

struct AttnState {
    bf16x8 q[4];
    f32x4 o[4][2];
    f32x4 l[4];
    float m[4];
};

template <bool PREV>
__device__ __forceinline__ void attn_max(const AttnState& st, float (&mx)[4], unsigned ka, int wm, f32x4 bias) {
    const f32x4 zero = {0.f, 0.f, 0.f, 0.f};
    const int first = PREV ? 4 * wm + 4 : 0, count = PREV ? 12 - 4 * wm : 4 * wm;
    unsigned a = ka + (unsigned)first * 1024u;
#pragma unroll 1
    for (int it = 0; it < count; it += 2, a += 2048u) {
        const bf16x8 k0 = lds_read16(a), k1 = lds_read16(a + 1024u);
#pragma unroll
        for (int qb = 0; qb < 4; ++qb) {
            mx[qb] = max4(mx[qb], mfma16(k0, st.q[qb], zero));
            mx[qb] = max4(mx[qb], mfma16(k1, st.q[qb], zero));
        }
    }
    a = ka + (unsigned)wm * 4096u;
#pragma unroll
    for (int t = 0; t < 4; ++t) {
        const bf16x8 k = lds_read16(a + t * 1024u);
#pragma unroll
        for (int qb = 0; qb < 4; ++qb) {
            if (t == qb) mx[qb] = max4(mx[qb], mfma16(k, st.q[qb], bias));
            else if (PREV ? t > qb : t < qb) mx[qb] = max4(mx[qb], mfma16(k, st.q[qb], zero));
        }
    }
}

__device__ __forceinline__ u32x2 exp_pack(f32x4 s) {
    u32x2 r;
    r[0] = pack2(__builtin_amdgcn_exp2f(s[0]), __builtin_amdgcn_exp2f(s[1]));
    r[1] = pack2(__builtin_amdgcn_exp2f(s[2]), __builtin_amdgcn_exp2f(s[3]));
    return r;
}
struct VFrag { bf16x8 d0, d1; };
__device__ __forceinline__ VFrag read_v(unsigned va0, unsigned va1, unsigned off) {
    VFrag v;
    const bf16x4 a = lds_read_tr(va0 + off), b = lds_read_tr(va0 + off + 1024u);
    const bf16x4 c = lds_read_tr(va1 + off), d = lds_read_tr(va1 + off + 1024u);
    v.d0 = __builtin_shufflevector(a, b, 0, 1, 2, 3, 4, 5, 6, 7);
    v.d1 = __builtin_shufflevector(c, d, 0, 1, 2, 3, 4, 5, 6, 7);
    return v;
}
template <int MODE0, int MODE1>
__device__ __forceinline__ void attn_pair(AttnState& st, int qb, bf16x8 k0, bf16x8 k1, const VFrag& v, f32x4 bias, bf16x8 ones) {
    const float nm = -st.m[qb];
    const f32x4 c = {nm, nm, nm, nm};
    u32x2 p0 = {0u, 0u}, p1 = {0u, 0u};
    if (MODE0) p0 = exp_pack(mfma16(k0, st.q[qb], MODE0 == 2 ? c + bias : c));
    if (MODE1) p1 = exp_pack(mfma16(k1, st.q[qb], MODE1 == 2 ? c + bias : c));
    const u32x4 pp = {p0[0], p0[1], p1[0], p1[1]};
    const bf16x8 p = as_frag(pp);
    st.o[qb][0] = mfma16(v.d0, p, st.o[qb][0]);
    st.o[qb][1] = mfma16(v.d1, p, st.o[qb][1]);
    st.l[qb] = mfma16(ones, p, st.l[qb]);
}
template <bool PREV>
__device__ __forceinline__ void attn_acc(AttnState& st, unsigned ka, unsigned va0, unsigned va1, int wm, f32x4 bias, bf16x8 ones) {
    const int first = PREV ? 4 * wm + 4 : 0, count = PREV ? 12 - 4 * wm : 4 * wm;
    unsigned off = (unsigned)first * 1024u;
#pragma unroll 1
    for (int it = 0; it < count; it += 2, off += 2048u) {
        const bf16x8 k0 = lds_read16(ka + off), k1 = lds_read16(ka + off + 1024u);
        const VFrag v = read_v(va0, va1, off);
#pragma unroll
        for (int qb = 0; qb < 4; ++qb) attn_pair<1, 1>(st, qb, k0, k1, v, bias, ones);
    }
    off = (unsigned)wm * 4096u;
    {
        const bf16x8 k0 = lds_read16(ka + off), k1 = lds_read16(ka + off + 1024u);
        const VFrag v = read_v(va0, va1, off);
        if (PREV) {
            attn_pair<2, 1>(st, 0, k0, k1, v, bias, ones);
            attn_pair<0, 2>(st, 1, k0, k1, v, bias, ones);
        } else {
            attn_pair<2, 0>(st, 0, k0, k1, v, bias, ones);
            attn_pair<1, 2>(st, 1, k0, k1, v, bias, ones);
            attn_pair<1, 1>(st, 2, k0, k1, v, bias, ones);
            attn_pair<1, 1>(st, 3, k0, k1, v, bias, ones);
        }
    }
    off += 2048u;
    {
        const bf16x8 k0 = lds_read16(ka + off), k1 = lds_read16(ka + off + 1024u);
        const VFrag v = read_v(va0, va1, off);
        if (PREV) {
            attn_pair<1, 1>(st, 0, k0, k1, v, bias, ones);
            attn_pair<1, 1>(st, 1, k0, k1, v, bias, ones);
            attn_pair<2, 1>(st, 2, k0, k1, v, bias, ones);
            attn_pair<0, 2>(st, 3, k0, k1, v, bias, ones);
        } else {
            attn_pair<2, 0>(st, 2, k0, k1, v, bias, ones);
            attn_pair<1, 2>(st, 3, k0, k1, v, bias, ones);
        }
    }
}

__device__ __forceinline__ void t64_tr(f32x32& acc0, f32x32& acc1, i32x16& fm, i32x16& fn, unsigned am0, unsigned am1,
                                       unsigned an0, unsigned an1, unsigned an2, unsigned an3) {
    int dl, slot = 0, n = 16;
    asm volatile(ASM_T64_TR
                 : "={v[0:31]}"(acc0), "={v[32:63]}"(acc1), "+{v[64:79]}"(fm), "+{v[80:95]}"(fn),
                   [am0] "+v"(am0), [am1] "+v"(am1), [an0] "+v"(an0), [an1] "+v"(an1), [an2] "+v"(an2), [an3] "+v"(an3),
                   [slot] "+s"(slot), [n] "+s"(n), [dl] "=&s"(dl)
                 : [z] "s"(1), [dwrap] "s"(-98304), [dstep] "s"(32768)
                 : "v96", "v97", "v98", "v99", "v100", "v101", "v102", "v103", "v104", "v105", "v106", "v107",
                   "v108", "v109", "v110", "v111", "v112", "v113", "v114", "v115", "v116", "v117", "v118", "v119",
                   "v120", "v121", "v122", "v123", "v124", "v125", "v126", "v127", "scc", "memory");
}

__device__ __forceinline__ void attn_consumer(int cw, int lane, char* ws, char* out, int w, int ib, int p) {
    const int wn = cw & 1, wm = cw < 4 ? (cw >> 1) : 3 - ((cw - 4) >> 1);
    if (cw == 0) {
        const u64 tag = flag_load(ws, CTL_EPOCH) + 1;
        if (lane == 0) {
            flag_store(ws, CTL_ARR + w, tag);
            *(volatile LAS u32x2*)(size_t)LDS_TAG = (u32x2){(unsigned)tag, (unsigned)(tag >> 32)};
            lds_set(LDS_CNT, 0u); lds_set(LDS_RDY_KV, 0u); lds_set(LDS_RDY_O, 0u);
        }
        wait_lgkm0();
    }
    AttnState st;
    int lr = lane & 15, g = lane >> 4, q4 = (lane >> 2) & 3, p4 = lane & 3;
    unsigned koff;
    {
        const unsigned am0 = (unsigned)((64 * wm + lr) * 128 + ((g ^ (lr >> 1)) * 16));
        const unsigned nsw = (unsigned)(((q4 >> 1) & 1) | ((g & 1) << 1));
        const unsigned anb = LDS_NRING + (unsigned)((8 * g + q4) * 128 + p4 * 8);
        const unsigned an0 = anb + (((unsigned)(2 * wn) ^ nsw) * 32u), an1 = anb + (((unsigned)(2 * wn + 1) ^ nsw) * 32u);
        f32x32 a0, a1, a2;
        wg_barrier();
        qkv_gemm(a0, a1, a2, am0, am0 ^ 64u, an0, an1);
        lane = fresh(lane);
        lr = lane & 15; g = lane >> 4; q4 = (lane >> 2) & 3; p4 = lane & 3;
        koff = (unsigned)(lr * 64 + ((g ^ ((-(lr >> 2)) & 3)) * 16));
#define ACC(i, j, r) ((24 * (i) + 4 * (j) + (r)) < 32 ? a0[(24 * (i) + 4 * (j) + (r)) & 31] : (24 * (i) + 4 * (j) + (r)) < 64 ? a1[(24 * (i) + 4 * (j) + (r)) & 31] : a2[(24 * (i) + 4 * (j) + (r)) & 31])
        const unsigned kw = (unsigned)(wn * 16384 + wm * 4096) + koff;
        const unsigned vw = (unsigned)(32768 + wn * 16384 + wm * 4096 + lr * 64 + ((((g & 1) ^ ((lr >> 2) & 1)) * 2 + (g >> 1)) * 16));
#pragma unroll
        for (int i = 0; i < 4; ++i) {
            u32x4 k = {pack2(ACC(i, 2, 0), ACC(i, 2, 1)), pack2(ACC(i, 2, 2), ACC(i, 2, 3)),
                       pack2(ACC(i, 3, 0), ACC(i, 3, 1)), pack2(ACC(i, 3, 2), ACC(i, 3, 3))};
            lds_write16(kw + i * 1024u, k);
            unsigned x0 = pack2(ACC(i, 4, 0), ACC(i, 4, 1)), x1 = pack2(ACC(i, 4, 2), ACC(i, 4, 3));
            unsigned y0 = pack2(ACC(i, 5, 0), ACC(i, 5, 1)), y1 = pack2(ACC(i, 5, 2), ACC(i, 5, 3));
            swap16(x0, y0); swap16(x1, y1);
            lds_write16(vw + i * 1024u, (u32x4){x0, x1, y0, y1});
            st.q[i] = as_frag((u32x4){pack2(ACC(i, 0, 0) * Q_SCALE, ACC(i, 0, 1) * Q_SCALE), pack2(ACC(i, 0, 2) * Q_SCALE, ACC(i, 0, 3) * Q_SCALE),
                                      pack2(ACC(i, 1, 0) * Q_SCALE, ACC(i, 1, 1) * Q_SCALE), pack2(ACC(i, 1, 2) * Q_SCALE, ACC(i, 1, 3) * Q_SCALE)});
        }
#undef ACC
    }
    wait_lgkm0();
    wg_barrier();
    const float ninf = -__builtin_inff();
    f32x4 bias_own, bias_prev;
#pragma unroll
    for (int r = 0; r < 4; ++r) {
        const bool le = 4 * g + r <= lr;
        bias_own[r] = le ? 0.f : ninf;
        bias_prev[r] = le ? ninf : 0.f;
    }
    const bf16x8 ones = as_frag((u32x4){0x3f803f80u, 0x3f803f80u, 0x3f803f80u, 0x3f803f80u});
    const unsigned ka = (unsigned)(wn * 16384) + koff;
    const unsigned vb = (unsigned)(32768 + wn * 16384 + (4 * g + q4) * 64 + p4 * 8);
    const unsigned va0 = vb + (unsigned)((g & 1) * 32), va1 = vb + (unsigned)(((g & 1) ^ 1) * 32);
    {
        float mx[4] = {ninf, ninf, ninf, ninf};
        attn_max<false>(st, mx, ka, wm, bias_own);
#pragma unroll
        for (int qb = 0; qb < 4; ++qb) {
            st.m[qb] = row_max(mx[qb]);
            st.o[qb][0] = st.o[qb][1] = st.l[qb] = (f32x4){0.f, 0.f, 0.f, 0.f};
        }
        attn_acc<false>(st, ka, va0, va1, wm, bias_own, ones);
    }
    wg_barrier();
    if (ib > 0) {
        float mx[4] = {st.m[0], st.m[1], st.m[2], st.m[3]};
        attn_max<true>(st, mx, ka + LDS_PREV, wm, bias_prev);
#pragma unroll
        for (int qb = 0; qb < 4; ++qb) {
            const float mn = row_max(mx[qb]);
            const float al = __builtin_amdgcn_exp2f(st.m[qb] - mn);
            st.m[qb] = mn;
            st.o[qb][0] *= al; st.o[qb][1] *= al; st.l[qb] *= al;
        }
        attn_acc<true>(st, ka + LDS_PREV, va0 + LDS_PREV, va1 + LDS_PREV, wm, bias_prev, ones);
    }
    lane = fresh(lane);
    lr = lane & 15; g = lane >> 4; q4 = (lane >> 2) & 3; p4 = lane & 3;
    {
        char* ob = ws + WS_O + (size_t)(ib * 16 + p) * 32768;
#pragma unroll
        for (int qb = 0; qb < 4; ++qb) {
            const float inv = 1.0f / st.l[qb][0];
            const int row = 64 * wm + 16 * qb + lr;
            u32x4 v = {pack2(st.o[qb][0][0] * inv, st.o[qb][0][1] * inv), pack2(st.o[qb][0][2] * inv, st.o[qb][0][3] * inv),
                       pack2(st.o[qb][1][0] * inv, st.o[qb][1][1] * inv), pack2(st.o[qb][1][2] * inv, st.o[qb][1][3] * inv)};
            store16_dev(ob, (unsigned)(row * 128 + (((4 * wn + g) ^ ((row >> 1) & 7)) * 16)), v);
        }
        wait_vm<0>();
    }
    wg_barrier();
    if (cw >= 4) return;
    {
        const int om = cw >> 1, on = cw & 1;
        const unsigned am0 = (unsigned)((64 * om + lr) * 128 + ((g ^ (lr >> 1)) * 16));
        const unsigned anb = 16384u + (unsigned)((8 * g + q4) * 256 + p4 * 8);
        const unsigned nsw = (unsigned)(q4 | ((g & 1) << 2));
        const unsigned an0 = anb + (((unsigned)(4 * on + 0) ^ nsw) * 32u), an1 = anb + (((unsigned)(4 * on + 1) ^ nsw) * 32u);
        const unsigned an2 = anb + (((unsigned)(4 * on + 2) ^ nsw) * 32u), an3 = anb + (((unsigned)(4 * on + 3) ^ nsw) * 32u);
        f32x32 acc0, acc1;
        i32x16 fm, fn;
        wg_barrier();
        asm volatile(
            "ds_read_b128 v[64:67], %2\n ds_read_b128 v[68:71], %2 offset:2048\n ds_read_b128 v[72:75], %2 offset:4096\n ds_read_b128 v[76:79], %2 offset:6144\n"
            "ds_read_b64_tr_b16 v[80:81], %3\n ds_read_b64_tr_b16 v[82:83], %3 offset:1024\n"
            "ds_read_b64_tr_b16 v[84:85], %4\n ds_read_b64_tr_b16 v[86:87], %4 offset:1024\n"
            "ds_read_b64_tr_b16 v[88:89], %5\n ds_read_b64_tr_b16 v[90:91], %5 offset:1024\n"
            "ds_read_b64_tr_b16 v[92:93], %6\n ds_read_b64_tr_b16 v[94:95], %6 offset:1024\n"
            "s_waitcnt lgkmcnt(0)\n"
            : "={v[64:79]}"(fm), "={v[80:95]}"(fn) : "v"(am0), "v"(an0), "v"(an1), "v"(an2), "v"(an3) : "memory");
        t64_tr(acc0, acc1, fm, fn, am0, am0 ^ 64u, an0, an1, an2, an3);
        char* ob = out + (size_t)(256 * ib + 128 * (p & 1) + 64 * om) * 4096 + (size_t)(128 * (p >> 1) + 64 * on) * 2;
        const unsigned vo = (unsigned)(lr * 4096 + ((g & 1) * 2 + (g >> 1)) * 16);
#pragma unroll
        for (int i = 0; i < 4; ++i) {
#pragma unroll
            for (int J = 0; J < 2; ++J) {
                const int r = (i & 1) * 16 + J * 8;
                const f32x32& a = (i < 2) ? acc0 : acc1;
                unsigned x0 = pack2(a[r + 0], a[r + 1]), x1 = pack2(a[r + 2], a[r + 3]);
                unsigned y0 = pack2(a[r + 4], a[r + 5]), y1 = pack2(a[r + 6], a[r + 7]);
                swap16(x0, y0); swap16(x1, y1);
                store16(ob, vo + i * 16u * 4096u + J * 64u, (u32x4){x0, x1, y0, y1});
            }
        }
    }
}

__device__ __forceinline__ void lds_wait_word(unsigned a) {
    for (int spin = 0; spin < SPIN_LIMIT && lds_word(a) == 0u; ++spin) __builtin_amdgcn_s_sleep(1);
}

__device__ __forceinline__ void attn_loader(int lw, int lane, const char* Ax, const char* Wqkv, const char* Wo, char* ws, int ib, int p) {
    {
        const unsigned swz = (unsigned)(((lane & 7) ^ (4 * (lw & 1) + (lane >> 4))) * 16);
        const unsigned vm = (unsigned)(8 * lw + (lane >> 3)) * 2048u + swz;
        const int r8 = lane >> 3, P = lane & 7;
        const unsigned c = (unsigned)((P >> 1) ^ (((r8 >> 1) & 1) | ((lw & 1) << 1)));
        const unsigned vn = (unsigned)(8 * lw + r8) * 6144u + c * 32u + (unsigned)(P & 1) * 16u;
        const i32x4 srdm = make_srd(Ax + (size_t)ib * 256 * 2048, 256 * 2048);
        const i32x4 srdn = make_srd(Wqkv + (size_t)p * 128, 1024 * 6144 - p * 128);
        const unsigned ldsw = (unsigned)lw * 1024u;
        auto issue_m = [&](int k) {
            const unsigned lds = (unsigned)(k % 3) * 32768u + ldsw;
#pragma unroll
            for (int i = 0; i < 8; ++i) dma16(vm, srdm, (unsigned)k * 128u + i * 32u * 2048u, lds + i * 4096u);
        };
        auto issue_n = [&](int k) {
            const unsigned lds = LDS_NRING + (unsigned)(k & 1) * 24576u + ldsw;
#pragma unroll
            for (int s = 0; s < 3; ++s)
#pragma unroll
                for (int i = 0; i < 2; ++i)
                    dma16(vn, srdn, (unsigned)k * 64u * 6144u + i * 32u * 6144u + s * 2048u, lds + s * 8192u + i * 4096u);
        };
        issue_n(0); issue_m(0); issue_n(1); issue_m(1); issue_m(2);
        wait_vm<22>();
        wg_barrier();
#pragma unroll 1
        for (int k = 0; k < 16; ++k) {

            if (k < 14) wait_vm<8>(); else wait_vm<0>();

            wg_barrier();

            if (k + 2 < 16) issue_n(k + 2);
            if (k + 3 < 16) issue_m(k + 3);

        }
    }
    wg_barrier();
    const u64 tag = lds_tag();
    if (ib < 7) {
        char* img = ws + WS_KV + (size_t)(ib * 16 + p) * 65536;
        const unsigned off = (unsigned)lw * 16384u + (unsigned)lane * 16u;
        u32x4 tmp[16];
#pragma unroll
        for (int i = 0; i < 16; ++i) tmp[i] = *(LAS u32x4*)(size_t)(off + i * 1024u);
#pragma unroll
        for (int i = 0; i < 16; ++i) store16_dev(img, off + i * 1024u, tmp[i]);
        wait_vm<0>();
        if (lane == 0) {
            const unsigned old = __hip_atomic_fetch_add((LAS unsigned*)(size_t)LDS_CNT, 1u, __ATOMIC_RELAXED, __HIP_MEMORY_SCOPE_WORKGROUP);
            if (old == 3u) flag_store(ws, CTL_KV + ib * 16 + p, tag);
        }
    }
    if (ib > 0) {
        if (lw == 0) {
            for (int spin = 0; spin < SPIN_LIMIT && flag_load(ws, CTL_KV + (ib - 1) * 16 + p) != tag; ++spin) __builtin_amdgcn_s_sleep(2);
            lds_set(LDS_RDY_KV, 1u);
        } else {
            lds_wait_word(LDS_RDY_KV);
        }
        const i32x4 srdk = make_srd(ws + WS_KV + (size_t)((ib - 1) * 16 + p) * 65536, 65536);
        const unsigned off = (unsigned)lw * 16384u;
#pragma unroll
        for (int i = 0; i < 16; ++i) dma16_dev((unsigned)lane * 16u, srdk, off + i * 1024u, LDS_PREV + off + i * 1024u);
        wait_vm<0>();
    }
    wg_barrier();
    wg_barrier();
    if (lw == 0 && lane == 0) flag_store(ws, CTL_FO + ib * 16 + p, tag);
    {
        const int r = lane >> 4, P = lane & 15;
        const unsigned c = (unsigned)((P >> 1) ^ (r | (((lw >> 1) & 1) << 2)));
        const unsigned vn = (unsigned)(16 * (lw & 1) + 4 * (lw >> 1) + r) * 2048u + c * 32u + (unsigned)(P & 1) * 16u;
        const i32x4 srdn = make_srd(Wo + (size_t)(p >> 1) * 256, 1024 * 2048 - (p >> 1) * 256);
        const i32x4 srdm = make_srd(ws + WS_O + (size_t)ib * 16 * 32768 + (size_t)(p & 1) * 16384, 16 * 32768 - (p & 1) * 16384);
        const unsigned ldsw = (unsigned)lw * 1024u, vm = (unsigned)lane * 16u + ldsw;
        auto issue_n = [&](int k) {
            const unsigned lds = (unsigned)(k & 3) * 32768u + 16384u + ldsw;
#pragma unroll
            for (int i = 0; i < 4; ++i)
                dma16(vn, srdn, ((unsigned)k * 64u + 32u * (i >> 1) + 8u * (i & 1)) * 2048u, lds + i * 4096u);
        };
        auto issue_m = [&](int k) {
            const unsigned lds = (unsigned)(k & 3) * 32768u + ldsw;
#pragma unroll
            for (int i = 0; i < 4; ++i) dma16_dev(vm, srdm, (unsigned)k * 32768u + i * 4096u, lds + i * 4096u);
        };
#pragma unroll
        for (int k = 0; k < 4; ++k) issue_n(k);
        if (lw == 0) {
            for (int spin = 0; spin < SPIN_LIMIT; ++spin) {
                if (__all(flag_load(ws, CTL_FO + ib * 16 + (lane & 15)) == tag)) break;
                __builtin_amdgcn_s_sleep(2);
            }
            lds_set(LDS_RDY_O, 1u);
        } else {
            lds_wait_word(LDS_RDY_O);
        }
#pragma unroll
        for (int k = 0; k < 4; ++k) issue_m(k);
        wait_vm<12>();
        wg_barrier();
#pragma unroll 1
        for (int k = 0; k < 16; ++k) {
            if (k == 0) wait_vm<8>(); else if (k == 1) wait_vm<12>(); else if (k < 13) wait_vm<16>(); else if (k == 13) wait_vm<8>(); else wait_vm<0>();
            wg_barrier();
            if (k + 4 < 16) { issue_n(k + 4); issue_m(k + 4); }
        }
    }
}

__global__ __launch_bounds__(768) void fused_kernel(const char* __restrict__ Ax, const char* __restrict__ Wqkv, const char* __restrict__ Wo,
                                                    const char* __restrict__ Bx, const char* __restrict__ Cr, const char* __restrict__ Ci,
                                                    const char* __restrict__ Dr, const char* __restrict__ Di, char* out, char* ws) {
    const int tid = threadIdx.x, lane = tid & 63, wid = uni(tid >> 6);
    const int w = blockIdx.x, x = w & 7, idx = w >> 3;
    if (idx < 16) {
        if (AONLY) { if (wid == 0) { const u64 tag = flag_load(ws, CTL_EPOCH) + 1; if (lane == 0) flag_store(ws, CTL_ARR + w, tag);
            if (w == 0) { for (int spin = 0; spin < SPIN_LIMIT; ++spin) { bool ok = true;
                for (int q = 0; q < 4; ++q) ok = ok && (flag_load(ws, CTL_ARR + q * 64 + lane) == tag);
                if (__all(ok)) break; __builtin_amdgcn_s_sleep(4); }
                if (lane == 0) flag_store(ws, CTL_EPOCH, tag); } }
            return; }
        const int eb = 4 * (x & 1) + (idx & 3), sb = 4 * (x >> 1) + (idx >> 2);
        const int tb = sb, eb3 = eb;
        if (wid < 4) spectral_consumer(wid, lane, ws, out, eb, sb, tb, eb3);
        else if (wid < 4 + NLOAD) spectral_loader(wid - 4, lane, Bx, Cr, Ci, Dr, Di, ws, eb, sb, tb, eb3, x);
        else if (wid == 8) spectral_control(lane, ws, w, eb, sb, eb3);
        else if (wid == 9 && PF_DIST > 0) spectral_prefetch(lane, Bx, Cr, Ci, Dr, Di, ws, idx, eb, sb, tb, eb3);
    } else {
        const int a = idx - 16, ib = 4 * (x & 1) + (a & 3), p = 4 * (x >> 1) + (a >> 2);
        if (BONLY) { if (wid == 0) { const u64 tag = flag_load(ws, CTL_EPOCH) + 1; if (lane == 0) flag_store(ws, CTL_ARR + w, tag); } return; }
        if (wid < 8) attn_consumer(wid, lane, ws, out, w, ib, p);
        else attn_loader(wid - 8, lane, Ax, Wqkv, Wo, ws, ib, p);
    }
}

extern "C" void kernel_launch(void* const* d_in, const int* in_sizes, int n_in, void* d_out, int out_size, void* d_ws, size_t ws_size,
                              hipStream_t stream) {
    (void)in_sizes; (void)n_in; (void)out_size; (void)ws_size;
    (void)hipFuncSetAttribute((const void*)fused_kernel, hipFuncAttributeMaxDynamicSharedMemorySize, LDS_BYTES);
    hipLaunchKernelGGL(fused_kernel, dim3(256), dim3(768), LDS_BYTES, stream, (const char*)d_in[0], (const char*)d_in[1],
                       (const char*)d_in[2], (const char*)d_in[3], (const char*)d_in[4], (const char*)d_in[5], (const char*)d_in[6],
                       (const char*)d_in[7], (char*)d_out, (char*)d_ws);
}
```

```cpp
#define XBF16 1
#define CODE_BYTES 32000
#define FULLLINE 1
#define PDELAY -1
#define ATT2 1
#define TOUCHC 5
#define EARLY 5
#define NPF 5
#define NMIX 1
#define TOUCH0 0
#define KROT 2
#define BDIR 2
#define BORD 2
#define ORING 4
#define OLD8 0
#define TCK 4
#define KVDIR 0
#define APRIO 0
#define OFIRST 0
#define MPF 0
#define KROTB 2
#define TOUCHW 0
#include <hip/hip_runtime.h>
#include <stdint.h>

#define GAS __attribute__((address_space(1)))
#define LAS __attribute__((address_space(3)))
typedef float f32x4 __attribute__((ext_vector_type(4)));
typedef float f32x32 __attribute__((ext_vector_type(32)));
typedef int i32x4 __attribute__((ext_vector_type(4)));
typedef int i32x8 __attribute__((ext_vector_type(8)));
typedef int i32x16 __attribute__((ext_vector_type(16)));
typedef unsigned u32x2 __attribute__((ext_vector_type(2)));
typedef unsigned u32x4 __attribute__((ext_vector_type(4)));
typedef __bf16 bf16x2 __attribute__((ext_vector_type(2)));
typedef __bf16 bf16x4 __attribute__((ext_vector_type(4)));
typedef __bf16 bf16x8 __attribute__((ext_vector_type(8)));
typedef unsigned long long u64;

#define ASM_T64_R3 \
    "ds_read_b128 v[80:83], %[an0] offset:0\n" \
    "ds_read_b128 v[64:67], %[am0] offset:0\n" \
    "ds_read_b128 v[84:87], %[an0] offset:2048\n" \
    "ds_read_b128 v[68:71], %[am0] offset:2048\n" \
    "ds_read_b128 v[88:91], %[an0] offset:4096\n" \
    "ds_read_b128 v[72:75], %[am0] offset:4096\n" \
    "ds_read_b128 v[92:95], %[an0] offset:6144\n" \
    "ds_read_b128 v[76:79], %[am0] offset:6144\n" \
    "s_waitcnt lgkmcnt(0)\n" \
    "v_mfma_f32_16x16x32_bf16 v[0:3], v[80:83], v[64:67], 0\n" \
    "v_mfma_f32_16x16x32_bf16 v[4:7], v[84:87], v[64:67], 0\n" \
    "ds_read_b128 v[112:115], %[an1] offset:0\n" \
    "v_mfma_f32_16x16x32_bf16 v[8:11], v[88:91], v[64:67], 0\n" \
    "ds_read_b128 v[96:99], %[am1] offset:0\n" \
    "v_mfma_f32_16x16x32_bf16 v[12:15], v[92:95], v[64:67], 0\n" \
    "v_mfma_f32_16x16x32_bf16 v[16:19], v[80:83], v[68:71], 0\n" \
    "ds_read_b128 v[116:119], %[an1] offset:2048\n" \
    "v_mfma_f32_16x16x32_bf16 v[20:23], v[84:87], v[68:71], 0\n" \
    "ds_read_b128 v[100:103], %[am1] offset:2048\n" \
    "v_mfma_f32_16x16x32_bf16 v[24:27], v[88:91], v[68:71], 0\n" \
    "v_mfma_f32_16x16x32_bf16 v[28:31], v[92:95], v[68:71], 0\n" \
    "ds_read_b128 v[120:123], %[an1] offset:4096\n" \
    "v_mfma_f32_16x16x32_bf16 v[32:35], v[80:83], v[72:75], 0\n" \
    "ds_read_b128 v[104:107], %[am1] offset:4096\n" \
    "v_mfma_f32_16x16x32_bf16 v[36:39], v[84:87], v[72:75], 0\n" \
    "v_mfma_f32_16x16x32_bf16 v[40:43], v[88:91], v[72:75], 0\n" \
    "ds_read_b128 v[124:127], %[an1] offset:6144\n" \
    "v_mfma_f32_16x16x32_bf16 v[44:47], v[92:95], v[72:75], 0\n" \
    "ds_read_b128 v[108:111], %[am1] offset:6144\n" \
    "v_mfma_f32_16x16x32_bf16 v[48:51], v[80:83], v[76:79], 0\n" \
    "v_mfma_f32_16x16x32_bf16 v[52:55], v[84:87], v[76:79], 0\n" \
    "s_add_u32 %[slot], %[slot], 1\n" \
    "s_cmp_eq_u32 %[slot], 3\n" \
    "s_cselect_b32 %[slot], 0, %[slot]\n" \
    "s_cselect_b32 %[dl], %[dwrap], %[dstep]\n" \
    "v_mfma_f32_16x16x32_bf16 v[56:59], v[88:91], v[76:79], 0\n" \
    "v_add_u32 %[am0], %[dl], %[am0]\n" \
    "v_add_u32 %[an0], %[dl], %[an0]\n" \
    "v_mfma_f32_16x16x32_bf16 v[60:63], v[92:95], v[76:79], 0\n" \
    "s_branch MID%=\n" \
    "TOP%=:\n" \
    "s_waitcnt lgkmcnt(0)\n" \
    "v_mfma_f32_16x16x32_bf16 v[0:3], v[80:83], v[64:67], v[0:3]\n" \
    "v_mfma_f32_16x16x32_bf16 v[4:7], v[84:87], v[64:67], v[4:7]\n" \
    "ds_read_b128 v[112:115], %[an1] offset:0\n" \
    "v_mfma_f32_16x16x32_bf16 v[8:11], v[88:91], v[64:67], v[8:11]\n" \
    "ds_read_b128 v[96:99], %[am1] offset:0\n" \
    "v_mfma_f32_16x16x32_bf16 v[12:15], v[92:95], v[64:67], v[12:15]\n" \
    "v_mfma_f32_16x16x32_bf16 v[16:19], v[80:83], v[68:71], v[16:19]\n" \
    "ds_read_b128 v[116:119], %[an1] offset:2048\n" \
    "v_mfma_f32_16x16x32_bf16 v[20:23], v[84:87], v[68:71], v[20:23]\n" \
    "ds_read_b128 v[100:103], %[am1] offset:2048\n" \
    "v_mfma_f32_16x16x32_bf16 v[24:27], v[88:91], v[68:71], v[24:27]\n" \
    "v_mfma_f32_16x16x32_bf16 v[28:31], v[92:95], v[68:71], v[28:31]\n" \
    "ds_read_b128 v[120:123], %[an1] offset:4096\n" \
    "v_mfma_f32_16x16x32_bf16 v[32:35], v[80:83], v[72:75], v[32:35]\n" \
    "ds_read_b128 v[104:107], %[am1] offset:4096\n" \
    "v_mfma_f32_16x16x32_bf16 v[36:39], v[84:87], v[72:75], v[36:39]\n" \
    "v_mfma_f32_16x16x32_bf16 v[40:43], v[88:91], v[72:75], v[40:43]\n" \
    "ds_read_b128 v[124:127], %[an1] offset:6144\n" \
    "v_mfma_f32_16x16x32_bf16 v[44:47], v[92:95], v[72:75], v[44:47]\n" \
    "ds_read_b128 v[108:111], %[am1] offset:6144\n" \
    "v_mfma_f32_16x16x32_bf16 v[48:51], v[80:83], v[76:79], v[48:51]\n" \
    "v_mfma_f32_16x16x32_bf16 v[52:55], v[84:87], v[76:79], v[52:55]\n" \
    "s_add_u32 %[slot], %[slot], 1\n" \
    "s_cmp_eq_u32 %[slot], 3\n" \
    "s_cselect_b32 %[slot], 0, %[slot]\n" \
    "s_cselect_b32 %[dl], %[dwrap], %[dstep]\n" \
    "v_mfma_f32_16x16x32_bf16 v[56:59], v[88:91], v[76:79], v[56:59]\n" \
    "v_add_u32 %[am0], %[dl], %[am0]\n" \
    "v_add_u32 %[an0], %[dl], %[an0]\n" \
    "v_mfma_f32_16x16x32_bf16 v[60:63], v[92:95], v[76:79], v[60:63]\n" \
    "MID%=:\n" \
    "s_waitcnt lgkmcnt(0)\n" \
    "s_barrier\n" \
    "v_mfma_f32_16x16x32_bf16 v[0:3], v[112:115], v[96:99], v[0:3]\n" \
    "v_mfma_f32_16x16x32_bf16 v[4:7], v[116:119], v[96:99], v[4:7]\n" \
    "ds_read_b128 v[80:83], %[an0] offset:0\n" \
    "v_mfma_f32_16x16x32_bf16 v[8:11], v[120:123], v[96:99], v[8:11]\n" \
    "ds_read_b128 v[64:67], %[am0] offset:0\n" \
    "v_mfma_f32_16x16x32_bf16 v[12:15], v[124:127], v[96:99], v[12:15]\n" \
    "v_mfma_f32_16x16x32_bf16 v[16:19], v[112:115], v[100:103], v[16:19]\n" \
    "ds_read_b128 v[84:87], %[an0] offset:2048\n" \
    "v_mfma_f32_16x16x32_bf16 v[20:23], v[116:119], v[100:103], v[20:23]\n" \
    "ds_read_b128 v[68:71], %[am0] offset:2048\n" \
    "v_mfma_f32_16x16x32_bf16 v[24:27], v[120:123], v[100:103], v[24:27]\n" \
    "v_mfma_f32_16x16x32_bf16 v[28:31], v[124:127], v[100:103], v[28:31]\n" \
    "ds_read_b128 v[88:91], %[an0] offset:4096\n" \
    "v_mfma_f32_16x16x32_bf16 v[32:35], v[112:115], v[104:107], v[32:35]\n" \
    "ds_read_b128 v[72:75], %[am0] offset:4096\n" \
    "v_mfma_f32_16x16x32_bf16 v[36:39], v[116:119], v[104:107], v[36:39]\n" \
    "v_mfma_f32_16x16x32_bf16 v[40:43], v[120:123], v[104:107], v[40:43]\n" \
    "ds_read_b128 v[92:95], %[an0] offset:6144\n" \
    "v_mfma_f32_16x16x32_bf16 v[44:47], v[124:127], v[104:107], v[44:47]\n" \
    "ds_read_b128 v[76:79], %[am0] offset:6144\n" \
    "v_mfma_f32_16x16x32_bf16 v[48:51], v[112:115], v[108:111], v[48:51]\n" \
    "v_mfma_f32_16x16x32_bf16 v[52:55], v[116:119], v[108:111], v[52:55]\n" \
    "v_mfma_f32_16x16x32_bf16 v[56:59], v[120:123], v[108:111], v[56:59]\n" \
    "v_add_u32 %[am1], %[dl], %[am1]\n" \
    "v_add_u32 %[an1], %[dl], %[an1]\n" \
    "s_sub_u32 %[n], %[n], 1\n" \
    "v_mfma_f32_16x16x32_bf16 v[60:63], v[124:127], v[108:111], v[60:63]\n" \
    "s_cmp_lg_u32 %[n], 0\n" \
    "s_cbranch_scc1 TOP%=\n" \
    "s_waitcnt lgkmcnt(0)\n" \
    "s_nop 15\n" \
    ""

#define ASM_T64_TR \
    "s_cmp_eq_u32 %[z], 0\n" \
    "s_cbranch_scc1 TOP%=\n" \
    "s_waitcnt lgkmcnt(0)\n" \
    "v_mfma_f32_16x16x32_bf16 v[0:3], v[80:83], v[64:67], 0\n" \
    "v_mfma_f32_16x16x32_bf16 v[4:7], v[84:87], v[64:67], 0\n" \
    "ds_read_b64_tr_b16 v[112:113], %[an0] offset:8192\n" \
    "v_mfma_f32_16x16x32_bf16 v[8:11], v[88:91], v[64:67], 0\n" \
    "ds_read_b64_tr_b16 v[114:115], %[an0] offset:9216\n" \
    "v_mfma_f32_16x16x32_bf16 v[12:15], v[92:95], v[64:67], 0\n" \
    "ds_read_b128 v[96:99], %[am1] offset:0\n" \
    "v_mfma_f32_16x16x32_bf16 v[16:19], v[80:83], v[68:71], 0\n" \
    "ds_read_b64_tr_b16 v[116:117], %[an1] offset:8192\n" \
    "v_mfma_f32_16x16x32_bf16 v[20:23], v[84:87], v[68:71], 0\n" \
    "ds_read_b64_tr_b16 v[118:119], %[an1] offset:9216\n" \
    "v_mfma_f32_16x16x32_bf16 v[24:27], v[88:91], v[68:71], 0\n" \
    "ds_read_b128 v[100:103], %[am1] offset:2048\n" \
    "v_mfma_f32_16x16x32_bf16 v[28:31], v[92:95], v[68:71], 0\n" \
    "ds_read_b64_tr_b16 v[120:121], %[an2] offset:8192\n" \
    "v_mfma_f32_16x16x32_bf16 v[32:35], v[80:83], v[72:75], 0\n" \
    "ds_read_b64_tr_b16 v[122:123], %[an2] offset:9216\n" \
    "v_mfma_f32_16x16x32_bf16 v[36:39], v[84:87], v[72:75], 0\n" \
    "ds_read_b128 v[104:107], %[am1] offset:4096\n" \
    "v_mfma_f32_16x16x32_bf16 v[40:43], v[88:91], v[72:75], 0\n" \
    "ds_read_b64_tr_b16 v[124:125], %[an3] offset:8192\n" \
    "v_mfma_f32_16x16x32_bf16 v[44:47], v[92:95], v[72:75], 0\n" \
    "ds_read_b64_tr_b16 v[126:127], %[an3] offset:9216\n" \
    "v_mfma_f32_16x16x32_bf16 v[48:51], v[80:83], v[76:79], 0\n" \
    "ds_read_b128 v[108:111], %[am1] offset:6144\n" \
    "v_mfma_f32_16x16x32_bf16 v[52:55], v[84:87], v[76:79], 0\n" \
    "s_add_u32 %[slot], %[slot], 1\n" \
    "s_cmp_eq_u32 %[slot], %[nring]\n" \
    "s_cselect_b32 %[slot], 0, %[slot]\n" \
    "s_cselect_b32 %[dl], %[dwrap], %[dstep]\n" \
    "v_mfma_f32_16x16x32_bf16 v[56:59], v[88:91], v[76:79], 0\n" \
    "v_add_u32 %[am0], %[dl], %[am0]\n" \
    "v_mfma_f32_16x16x32_bf16 v[60:63], v[92:95], v[76:79], 0\n" \
    "v_add_u32 %[an0], %[dl], %[an0]\n" \
    "v_add_u32 %[an1], %[dl], %[an1]\n" \
    "v_add_u32 %[an2], %[dl], %[an2]\n" \
    "v_add_u32 %[an3], %[dl], %[an3]\n" \
    "s_branch MID%=\n" \
    "TOP%=:\n" \
    "s_waitcnt lgkmcnt(0)\n" \
    "v_mfma_f32_16x16x32_bf16 v[0:3], v[80:83], v[64:67], v[0:3]\n" \
    "v_mfma_f32_16x16x32_bf16 v[4:7], v[84:87], v[64:67], v[4:7]\n" \
    "ds_read_b64_tr_b16 v[112:113], %[an0] offset:8192\n" \
    "v_mfma_f32_16x16x32_bf16 v[8:11], v[88:91], v[64:67], v[8:11]\n" \
    "ds_read_b64_tr_b16 v[114:115], %[an0] offset:9216\n" \
    "v_mfma_f32_16x16x32_bf16 v[12:15], v[92:95], v[64:67], v[12:15]\n" \
    "ds_read_b128 v[96:99], %[am1] offset:0\n" \
    "v_mfma_f32_16x16x32_bf16 v[16:19], v[80:83], v[68:71], v[16:19]\n" \
    "ds_read_b64_tr_b16 v[116:117], %[an1] offset:8192\n" \
    "v_mfma_f32_16x16x32_bf16 v[20:23], v[84:87], v[68:71], v[20:23]\n" \
    "ds_read_b64_tr_b16 v[118:119], %[an1] offset:9216\n" \
    "v_mfma_f32_16x16x32_bf16 v[24:27], v[88:91], v[68:71], v[24:27]\n" \
    "ds_read_b128 v[100:103], %[am1] offset:2048\n" \
    "v_mfma_f32_16x16x32_bf16 v[28:31], v[92:95], v[68:71], v[28:31]\n" \
    "ds_read_b64_tr_b16 v[120:121], %[an2] offset:8192\n" \
    "v_mfma_f32_16x16x32_bf16 v[32:35], v[80:83], v[72:75], v[32:35]\n" \
    "ds_read_b64_tr_b16 v[122:123], %[an2] offset:9216\n" \
    "v_mfma_f32_16x16x32_bf16 v[36:39], v[84:87], v[72:75], v[36:39]\n" \
    "ds_read_b128 v[104:107], %[am1] offset:4096\n" \
    "v_mfma_f32_16x16x32_bf16 v[40:43], v[88:91], v[72:75], v[40:43]\n" \
    "ds_read_b64_tr_b16 v[124:125], %[an3] offset:8192\n" \
    "v_mfma_f32_16x16x32_bf16 v[44:47], v[92:95], v[72:75], v[44:47]\n" \
    "ds_read_b64_tr_b16 v[126:127], %[an3] offset:9216\n" \
    "v_mfma_f32_16x16x32_bf16 v[48:51], v[80:83], v[76:79], v[48:51]\n" \
    "ds_read_b128 v[108:111], %[am1] offset:6144\n" \
    "v_mfma_f32_16x16x32_bf16 v[52:55], v[84:87], v[76:79], v[52:55]\n" \
    "s_add_u32 %[slot], %[slot], 1\n" \
    "s_cmp_eq_u32 %[slot], %[nring]\n" \
    "s_cselect_b32 %[slot], 0, %[slot]\n" \
    "s_cselect_b32 %[dl], %[dwrap], %[dstep]\n" \
    "v_mfma_f32_16x16x32_bf16 v[56:59], v[88:91], v[76:79], v[56:59]\n" \
    "v_add_u32 %[am0], %[dl], %[am0]\n" \
    "v_mfma_f32_16x16x32_bf16 v[60:63], v[92:95], v[76:79], v[60:63]\n" \
    "v_add_u32 %[an0], %[dl], %[an0]\n" \
    "v_add_u32 %[an1], %[dl], %[an1]\n" \
    "v_add_u32 %[an2], %[dl], %[an2]\n" \
    "v_add_u32 %[an3], %[dl], %[an3]\n" \
    "MID%=:\n" \
    "s_waitcnt lgkmcnt(0)\n" \
    "s_barrier\n" \
    "v_mfma_f32_16x16x32_bf16 v[0:3], v[112:115], v[96:99], v[0:3]\n" \
    "v_mfma_f32_16x16x32_bf16 v[4:7], v[116:119], v[96:99], v[4:7]\n" \
    "ds_read_b64_tr_b16 v[80:81], %[an0] offset:0\n" \
    "v_mfma_f32_16x16x32_bf16 v[8:11], v[120:123], v[96:99], v[8:11]\n" \
    "ds_read_b64_tr_b16 v[82:83], %[an0] offset:1024\n" \
    "v_mfma_f32_16x16x32_bf16 v[12:15], v[124:127], v[96:99], v[12:15]\n" \
    "ds_read_b128 v[64:67], %[am0] offset:0\n" \
    "v_mfma_f32_16x16x32_bf16 v[16:19], v[112:115], v[100:103], v[16:19]\n" \
    "ds_read_b64_tr_b16 v[84:85], %[an1] offset:0\n" \
    "v_mfma_f32_16x16x32_bf16 v[20:23], v[116:119], v[100:103], v[20:23]\n" \
    "ds_read_b64_tr_b16 v[86:87], %[an1] offset:1024\n" \
    "v_mfma_f32_16x16x32_bf16 v[24:27], v[120:123], v[100:103], v[24:27]\n" \
    "ds_read_b128 v[68:71], %[am0] offset:2048\n" \
    "v_mfma_f32_16x16x32_bf16 v[28:31], v[124:127], v[100:103], v[28:31]\n" \
    "ds_read_b64_tr_b16 v[88:89], %[an2] offset:0\n" \
    "v_mfma_f32_16x16x32_bf16 v[32:35], v[112:115], v[104:107], v[32:35]\n" \
    "ds_read_b64_tr_b16 v[90:91], %[an2] offset:1024\n" \
    "v_mfma_f32_16x16x32_bf16 v[36:39], v[116:119], v[104:107], v[36:39]\n" \
    "ds_read_b128 v[72:75], %[am0] offset:4096\n" \
    "v_mfma_f32_16x16x32_bf16 v[40:43], v[120:123], v[104:107], v[40:43]\n" \
    "ds_read_b64_tr_b16 v[92:93], %[an3] offset:0\n" \
    "v_mfma_f32_16x16x32_bf16 v[44:47], v[124:127], v[104:107], v[44:47]\n" \
    "ds_read_b64_tr_b16 v[94:95], %[an3] offset:1024\n" \
    "v_mfma_f32_16x16x32_bf16 v[48:51], v[112:115], v[108:111], v[48:51]\n" \
    "ds_read_b128 v[76:79], %[am0] offset:6144\n" \
    "v_mfma_f32_16x16x32_bf16 v[52:55], v[116:119], v[108:111], v[52:55]\n" \
    "v_mfma_f32_16x16x32_bf16 v[56:59], v[120:123], v[108:111], v[56:59]\n" \
    "v_add_u32 %[am1], %[dl], %[am1]\n" \
    "s_sub_u32 %[n], %[n], 1\n" \
    "v_mfma_f32_16x16x32_bf16 v[60:63], v[124:127], v[108:111], v[60:63]\n" \
    "s_cmp_lg_u32 %[n], 0\n" \
    "s_cbranch_scc1 TOP%=\n" \
    "s_waitcnt lgkmcnt(0)\n" \
    "s_nop 15\n" \
    ""

#define ASM_QKV \
    "ds_read_b64_tr_b16 v[128:129], %[an0] offset:0\n" \
    "ds_read_b64_tr_b16 v[130:131], %[an0] offset:512\n" \
    "ds_read_b64_tr_b16 v[132:133], %[an1] offset:0\n" \
    "ds_read_b64_tr_b16 v[134:135], %[an1] offset:512\n" \
    "ds_read_b64_tr_b16 v[136:137], %[an0] offset:8192\n" \
    "ds_read_b64_tr_b16 v[138:139], %[an0] offset:8704\n" \
    "ds_read_b128 v[96:99], %[am0] offset:0\n" \
    "ds_read_b128 v[100:103], %[am0] offset:2048\n" \
    "ds_read_b64_tr_b16 v[140:141], %[an1] offset:8192\n" \
    "ds_read_b64_tr_b16 v[142:143], %[an1] offset:8704\n" \
    "ds_read_b128 v[104:107], %[am0] offset:4096\n" \
    "ds_read_b128 v[108:111], %[am0] offset:6144\n" \
    "ds_read_b64_tr_b16 v[144:145], %[an0] offset:16384\n" \
    "ds_read_b64_tr_b16 v[146:147], %[an0] offset:16896\n" \
    "s_waitcnt lgkmcnt(2)\n" \
    "v_mfma_f32_16x16x32_bf16 v[0:3], v[128:131], v[96:99], 0\n" \
    "ds_read_b64_tr_b16 v[148:149], %[an1] offset:16384\n" \
    "v_mfma_f32_16x16x32_bf16 v[24:27], v[128:131], v[100:103], 0\n" \
    "ds_read_b64_tr_b16 v[150:151], %[an1] offset:16896\n" \
    "v_mfma_f32_16x16x32_bf16 v[48:51], v[128:131], v[104:107], 0\n" \
    "v_mfma_f32_16x16x32_bf16 v[72:75], v[128:131], v[108:111], 0\n" \
    "v_mfma_f32_16x16x32_bf16 v[4:7], v[132:135], v[96:99], 0\n" \
    "ds_read_b64_tr_b16 v[128:129], %[an0] offset:4096\n" \
    "v_mfma_f32_16x16x32_bf16 v[28:31], v[132:135], v[100:103], 0\n" \
    "ds_read_b64_tr_b16 v[130:131], %[an0] offset:4608\n" \
    "v_mfma_f32_16x16x32_bf16 v[52:55], v[132:135], v[104:107], 0\n" \
    "ds_read_b128 v[112:115], %[am1] offset:0\n" \
    "v_mfma_f32_16x16x32_bf16 v[76:79], v[132:135], v[108:111], 0\n" \
    "v_mfma_f32_16x16x32_bf16 v[8:11], v[136:139], v[96:99], 0\n" \
    "ds_read_b64_tr_b16 v[132:133], %[an1] offset:4096\n" \
    "v_mfma_f32_16x16x32_bf16 v[32:35], v[136:139], v[100:103], 0\n" \
    "ds_read_b64_tr_b16 v[134:135], %[an1] offset:4608\n" \
    "v_mfma_f32_16x16x32_bf16 v[56:59], v[136:139], v[104:107], 0\n" \
    "ds_read_b128 v[116:119], %[am1] offset:2048\n" \
    "v_mfma_f32_16x16x32_bf16 v[80:83], v[136:139], v[108:111], 0\n" \
    "v_mfma_f32_16x16x32_bf16 v[12:15], v[140:143], v[96:99], 0\n" \
    "ds_read_b64_tr_b16 v[136:137], %[an0] offset:12288\n" \
    "v_mfma_f32_16x16x32_bf16 v[36:39], v[140:143], v[100:103], 0\n" \
    "ds_read_b64_tr_b16 v[138:139], %[an0] offset:12800\n" \
    "v_mfma_f32_16x16x32_bf16 v[60:63], v[140:143], v[104:107], 0\n" \
    "ds_read_b128 v[120:123], %[am1] offset:4096\n" \
    "v_mfma_f32_16x16x32_bf16 v[84:87], v[140:143], v[108:111], 0\n" \
    "s_waitcnt lgkmcnt(11)\n" \
    "v_mfma_f32_16x16x32_bf16 v[16:19], v[144:147], v[96:99], 0\n" \
    "ds_read_b64_tr_b16 v[140:141], %[an1] offset:12288\n" \
    "v_mfma_f32_16x16x32_bf16 v[40:43], v[144:147], v[100:103], 0\n" \
    "ds_read_b64_tr_b16 v[142:143], %[an1] offset:12800\n" \
    "v_mfma_f32_16x16x32_bf16 v[64:67], v[144:147], v[104:107], 0\n" \
    "ds_read_b128 v[124:127], %[am1] offset:6144\n" \
    "v_mfma_f32_16x16x32_bf16 v[88:91], v[144:147], v[108:111], 0\n" \
    "s_waitcnt lgkmcnt(12)\n" \
    "v_mfma_f32_16x16x32_bf16 v[20:23], v[148:151], v[96:99], 0\n" \
    "ds_read_b64_tr_b16 v[144:145], %[an0] offset:20480\n" \
    "v_mfma_f32_16x16x32_bf16 v[44:47], v[148:151], v[100:103], 0\n" \
    "ds_read_b64_tr_b16 v[146:147], %[an0] offset:20992\n" \
    "v_mfma_f32_16x16x32_bf16 v[68:71], v[148:151], v[104:107], 0\n" \
    "s_add_u32 %[ms], %[ms], 1\n" \
    "s_cmp_eq_u32 %[ms], 3\n" \
    "s_cselect_b32 %[ms], 0, %[ms]\n" \
    "s_cmp_eq_u32 %[ms], 0\n" \
    "s_cselect_b32 %[dm], %[mwrap], %[mstep]\n" \
    "v_mfma_f32_16x16x32_bf16 v[92:95], v[148:151], v[108:111], 0\n" \
    "s_xor_b32 %[ns], %[ns], 1\n" \
    "s_cmp_eq_u32 %[ns], 0\n" \
    "s_cselect_b32 %[dn], %[nwrap], %[nstep]\n" \
    "s_branch MID%=\n" \
    "TOP%=:\n" \
    "s_waitcnt lgkmcnt(2)\n" \
    "v_mfma_f32_16x16x32_bf16 v[0:3], v[128:131], v[96:99], v[0:3]\n" \
    "ds_read_b64_tr_b16 v[148:149], %[an1] offset:16384\n" \
    "v_mfma_f32_16x16x32_bf16 v[24:27], v[128:131], v[100:103], v[24:27]\n" \
    "ds_read_b64_tr_b16 v[150:151], %[an1] offset:16896\n" \
    "v_mfma_f32_16x16x32_bf16 v[48:51], v[128:131], v[104:107], v[48:51]\n" \
    "v_mfma_f32_16x16x32_bf16 v[72:75], v[128:131], v[108:111], v[72:75]\n" \
    "v_mfma_f32_16x16x32_bf16 v[4:7], v[132:135], v[96:99], v[4:7]\n" \
    "ds_read_b64_tr_b16 v[128:129], %[an0] offset:4096\n" \
    "v_mfma_f32_16x16x32_bf16 v[28:31], v[132:135], v[100:103], v[28:31]\n" \
    "ds_read_b64_tr_b16 v[130:131], %[an0] offset:4608\n" \
    "v_mfma_f32_16x16x32_bf16 v[52:55], v[132:135], v[104:107], v[52:55]\n" \
    "ds_read_b128 v[112:115], %[am1] offset:0\n" \
    "v_mfma_f32_16x16x32_bf16 v[76:79], v[132:135], v[108:111], v[76:79]\n" \
    "v_mfma_f32_16x16x32_bf16 v[8:11], v[136:139], v[96:99], v[8:11]\n" \
    "ds_read_b64_tr_b16 v[132:133], %[an1] offset:4096\n" \
    "v_mfma_f32_16x16x32_bf16 v[32:35], v[136:139], v[100:103], v[32:35]\n" \
    "ds_read_b64_tr_b16 v[134:135], %[an1] offset:4608\n" \
    "v_mfma_f32_16x16x32_bf16 v[56:59], v[136:139], v[104:107], v[56:59]\n" \
    "ds_read_b128 v[116:119], %[am1] offset:2048\n" \
    "v_mfma_f32_16x16x32_bf16 v[80:83], v[136:139], v[108:111], v[80:83]\n" \
    "v_mfma_f32_16x16x32_bf16 v[12:15], v[140:143], v[96:99], v[12:15]\n" \
    "ds_read_b64_tr_b16 v[136:137], %[an0] offset:12288\n" \
    "v_mfma_f32_16x16x32_bf16 v[36:39], v[140:143], v[100:103], v[36:39]\n" \
    "ds_read_b64_tr_b16 v[138:139], %[an0] offset:12800\n" \
    "v_mfma_f32_16x16x32_bf16 v[60:63], v[140:143], v[104:107], v[60:63]\n" \
    "ds_read_b128 v[120:123], %[am1] offset:4096\n" \
    "v_mfma_f32_16x16x32_bf16 v[84:87], v[140:143], v[108:111], v[84:87]\n" \
    "s_waitcnt lgkmcnt(11)\n" \
    "v_mfma_f32_16x16x32_bf16 v[16:19], v[144:147], v[96:99], v[16:19]\n" \
    "ds_read_b64_tr_b16 v[140:141], %[an1] offset:12288\n" \
    "v_mfma_f32_16x16x32_bf16 v[40:43], v[144:147], v[100:103], v[40:43]\n" \
    "ds_read_b64_tr_b16 v[142:143], %[an1] offset:12800\n" \
    "v_mfma_f32_16x16x32_bf16 v[64:67], v[144:147], v[104:107], v[64:67]\n" \
    "ds_read_b128 v[124:127], %[am1] offset:6144\n" \
    "v_mfma_f32_16x16x32_bf16 v[88:91], v[144:147], v[108:111], v[88:91]\n" \
    "s_waitcnt lgkmcnt(12)\n" \
    "v_mfma_f32_16x16x32_bf16 v[20:23], v[148:151], v[96:99], v[20:23]\n" \
    "ds_read_b64_tr_b16 v[144:145], %[an0] offset:20480\n" \
    "v_mfma_f32_16x16x32_bf16 v[44:47], v[148:151], v[100:103], v[44:47]\n" \
    "ds_read_b64_tr_b16 v[146:147], %[an0] offset:20992\n" \
    "v_mfma_f32_16x16x32_bf16 v[68:71], v[148:151], v[104:107], v[68:71]\n" \
    "s_add_u32 %[ms], %[ms], 1\n" \
    "s_cmp_eq_u32 %[ms], 3\n" \
    "s_cselect_b32 %[ms], 0, %[ms]\n" \
    "s_cmp_eq_u32 %[ms], 0\n" \
    "s_cselect_b32 %[dm], %[mwrap], %[mstep]\n" \
    "v_mfma_f32_16x16x32_bf16 v[92:95], v[148:151], v[108:111], v[92:95]\n" \
    "s_xor_b32 %[ns], %[ns], 1\n" \
    "s_cmp_eq_u32 %[ns], 0\n" \
    "s_cselect_b32 %[dn], %[nwrap], %[nstep]\n" \
    "MID%=:\n" \
    "s_waitcnt lgkmcnt(2)\n" \
    "v_mfma_f32_16x16x32_bf16 v[0:3], v[128:131], v[112:115], v[0:3]\n" \
    "ds_read_b64_tr_b16 v[148:149], %[an1] offset:20480\n" \
    "v_mfma_f32_16x16x32_bf16 v[24:27], v[128:131], v[116:119], v[24:27]\n" \
    "ds_read_b64_tr_b16 v[150:151], %[an1] offset:20992\n" \
    "v_mfma_f32_16x16x32_bf16 v[48:51], v[128:131], v[120:123], v[48:51]\n" \
    "v_add_u32 %[am0], %[dm], %[am0]\n" \
    "v_mfma_f32_16x16x32_bf16 v[72:75], v[128:131], v[124:127], v[72:75]\n" \
    "v_add_u32 %[am1], %[dm], %[am1]\n" \
    "v_mfma_f32_16x16x32_bf16 v[4:7], v[132:135], v[112:115], v[4:7]\n" \
    "v_add_u32 %[an0], %[dn], %[an0]\n" \
    "v_mfma_f32_16x16x32_bf16 v[28:31], v[132:135], v[116:119], v[28:31]\n" \
    "v_add_u32 %[an1], %[dn], %[an1]\n" \
    "v_mfma_f32_16x16x32_bf16 v[52:55], v[132:135], v[120:123], v[52:55]\n" \
    "v_mfma_f32_16x16x32_bf16 v[76:79], v[132:135], v[124:127], v[76:79]\n" \
    "s_waitcnt lgkmcnt(0)\n" \
    "s_barrier\n" \
    "v_mfma_f32_16x16x32_bf16 v[8:11], v[136:139], v[112:115], v[8:11]\n" \
    "ds_read_b64_tr_b16 v[128:129], %[an0] offset:0\n" \
    "v_mfma_f32_16x16x32_bf16 v[32:35], v[136:139], v[116:119], v[32:35]\n" \
    "ds_read_b64_tr_b16 v[130:131], %[an0] offset:512\n" \
    "v_mfma_f32_16x16x32_bf16 v[56:59], v[136:139], v[120:123], v[56:59]\n" \
    "ds_read_b64_tr_b16 v[132:133], %[an1] offset:0\n" \
    "v_mfma_f32_16x16x32_bf16 v[80:83], v[136:139], v[124:127], v[80:83]\n" \
    "ds_read_b64_tr_b16 v[134:135], %[an1] offset:512\n" \
    "v_mfma_f32_16x16x32_bf16 v[12:15], v[140:143], v[112:115], v[12:15]\n" \
    "ds_read_b64_tr_b16 v[136:137], %[an0] offset:8192\n" \
    "v_mfma_f32_16x16x32_bf16 v[36:39], v[140:143], v[116:119], v[36:39]\n" \
    "ds_read_b64_tr_b16 v[138:139], %[an0] offset:8704\n" \
    "v_mfma_f32_16x16x32_bf16 v[60:63], v[140:143], v[120:123], v[60:63]\n" \
    "ds_read_b128 v[96:99], %[am0] offset:0\n" \
    "v_mfma_f32_16x16x32_bf16 v[84:87], v[140:143], v[124:127], v[84:87]\n" \
    "ds_read_b128 v[100:103], %[am0] offset:2048\n" \
    "v_mfma_f32_16x16x32_bf16 v[16:19], v[144:147], v[112:115], v[16:19]\n" \
    "ds_read_b64_tr_b16 v[140:141], %[an1] offset:8192\n" \
    "v_mfma_f32_16x16x32_bf16 v[40:43], v[144:147], v[116:119], v[40:43]\n" \
    "ds_read_b64_tr_b16 v[142:143], %[an1] offset:8704\n" \
    "v_mfma_f32_16x16x32_bf16 v[64:67], v[144:147], v[120:123], v[64:67]\n" \
    "ds_read_b128 v[104:107], %[am0] offset:4096\n" \
    "v_mfma_f32_16x16x32_bf16 v[88:91], v[144:147], v[124:127], v[88:91]\n" \
    "ds_read_b128 v[108:111], %[am0] offset:6144\n" \
    "v_mfma_f32_16x16x32_bf16 v[20:23], v[148:151], v[112:115], v[20:23]\n" \
    "ds_read_b64_tr_b16 v[144:145], %[an0] offset:16384\n" \
    "v_mfma_f32_16x16x32_bf16 v[44:47], v[148:151], v[116:119], v[44:47]\n" \
    "ds_read_b64_tr_b16 v[146:147], %[an0] offset:16896\n" \
    "v_mfma_f32_16x16x32_bf16 v[68:71], v[148:151], v[120:123], v[68:71]\n" \
    "s_sub_u32 %[n], %[n], 1\n" \
    "v_mfma_f32_16x16x32_bf16 v[92:95], v[148:151], v[124:127], v[92:95]\n" \
    "s_cmp_lg_u32 %[n], 0\n" \
    "s_cbranch_scc1 TOP%=\n" \
    "s_waitcnt lgkmcnt(0)\n" \
    "s_nop 15\n" \
    ""

static constexpr size_t WS_ABT = 0;
static constexpr size_t WS_O = 8u << 20;
static constexpr size_t WS_KV = 12u << 20;
static constexpr size_t WS_X = 32u << 20;
static constexpr size_t WS_CTL = 24u << 20;
static constexpr int CTL_EPOCH = 0;
static constexpr int CTL_ARR = 1;
static constexpr int CTL_FA = CTL_ARR + 256;
static constexpr int CTL_KV = CTL_FA + 128;
static constexpr int CTL_FO = CTL_KV + 128;
static constexpr int CTL_FB = CTL_FO + 128;

static constexpr int LDS_BYTES = 160 * 1024;
static constexpr int SPIN_LIMIT = 1 << 17;


__device__ __forceinline__ void wg_barrier() { asm volatile("s_barrier" ::: "memory"); }
__device__ __forceinline__ int uni(int v) { return __builtin_amdgcn_readfirstlane(v); }
__device__ __forceinline__ int fresh(int v) { asm volatile("" : "+v"(v)); return v; }

__device__ __forceinline__ i32x4 make_srd(const void* p, unsigned bytes) {
    i32x4 r;
    r[0] = uni((int)(unsigned)(size_t)p);
    r[1] = uni((int)(unsigned)((size_t)p >> 32));
    r[2] = (int)bytes;
    r[3] = 0x00020000;
    return r;
}
__device__ __forceinline__ void dma16(unsigned voff, i32x4 srd, unsigned soff, unsigned lds) {
    asm volatile("s_mov_b32 m0, %3\n\ts_nop 0\n\tbuffer_load_dwordx4 %0, %1, %2 offen lds"
                 :: "v"(voff), "s"(srd), "s"(soff), "s"(lds) : "memory");
}
__device__ __forceinline__ void dma16_dev(unsigned voff, i32x4 srd, unsigned soff, unsigned lds) {
    asm volatile("s_mov_b32 m0, %3\n\ts_nop 0\n\tbuffer_load_dwordx4 %0, %1, %2 offen sc1 lds"
                 :: "v"(voff), "s"(srd), "s"(soff), "s"(lds) : "memory");
}
template <int N> __device__ __forceinline__ void wait_vm() { asm volatile("s_waitcnt vmcnt(%0)" :: "n"(N) : "memory"); }
__device__ __forceinline__ void wait_lgkm0() { asm volatile("s_waitcnt lgkmcnt(0)" ::: "memory"); }
__device__ __forceinline__ void acquire_agent() { asm volatile("buffer_inv sc1" ::: "memory"); }
__device__ __forceinline__ unsigned pack2(float a, float b) {
    bf16x2 v = {(__bf16)a, (__bf16)b};
    return __builtin_bit_cast(unsigned, v);
}
__device__ __forceinline__ void store16_dev(char* base, unsigned voff, u32x4 v) {
    asm volatile("global_store_dwordx4 %0, %1, %2 sc1\n\ts_nop 1" :: "v"(voff), "v"(v), "s"(base) : "memory");
}
__device__ __forceinline__ void store16(char* base, unsigned voff, u32x4 v) {
    asm volatile("global_store_dwordx4 %0, %1, %2 sc1\n\ts_nop 1" :: "v"(voff), "v"(v), "s"(base) : "memory");
}
__device__ __forceinline__ void pair_rows(u32x4& a, u32x4& b, bool low) {
#pragma unroll
    for (int r = 0; r < 4; ++r) {
        const unsigned ra = (unsigned)__builtin_amdgcn_update_dpp(0, (int)a[r], 0x128, 0xf, 0xf, true);
        const unsigned rb = (unsigned)__builtin_amdgcn_update_dpp(0, (int)b[r], 0x128, 0xf, 0xf, true);
        const unsigned na = low ? a[r] : rb, nb = low ? ra : b[r];
        a[r] = na; b[r] = nb;
    }
}
__device__ __forceinline__ GAS u64* ctl_line(char* ws, int line) { return (GAS u64*)(ws + WS_CTL + (size_t)line * 128); }
__device__ __forceinline__ void flag_store(char* ws, int line, u64 tag) {
    __hip_atomic_store(ctl_line(ws, line), tag, __ATOMIC_RELAXED, __HIP_MEMORY_SCOPE_AGENT);
}
__device__ __forceinline__ u64 flag_load(char* ws, int line) {
    return __hip_atomic_load(ctl_line(ws, line), __ATOMIC_RELAXED, __HIP_MEMORY_SCOPE_AGENT);
}

static constexpr unsigned LDS_DUMMY = 150 * 1024;
__device__ __forceinline__ void touch8k(const char* base, unsigned bytes, unsigned off, int lane) {
    const i32x4 srd = make_srd(base, bytes);
    asm volatile("s_mov_b32 m0, %3\n\ts_nop 0\n\tbuffer_load_dword %0, %1, %2 offen lds"
                 :: "v"((unsigned)lane * 128u), "s"(srd), "s"(off), "s"(LDS_DUMMY) : "memory");
}

__device__ __forceinline__ void touch4k(const char* base, unsigned bytes, unsigned off, int lane) {
    const i32x4 srd = make_srd(base, bytes);
    asm volatile("s_mov_b32 m0, %3\n\ts_nop 0\n\tbuffer_load_dword %0, %1, %2 offen lds"
                 :: "v"((unsigned)(lane & 31) * 128u), "s"(srd), "s"(off), "s"(LDS_DUMMY) : "memory");
}
static constexpr unsigned LDS_WORDS = 156 * 1024;
static constexpr unsigned LDS_TAG = LDS_WORDS, LDS_CNT = LDS_WORDS + 8, LDS_RDY_KV = LDS_WORDS + 12, LDS_RDY_O = LDS_WORDS + 16;
__device__ __forceinline__ unsigned lds_word(unsigned a) { return *(volatile LAS unsigned*)(size_t)a; }
__device__ __forceinline__ void lds_set(unsigned a, unsigned v) { *(volatile LAS unsigned*)(size_t)a = v; }
__device__ __forceinline__ u64 lds_tag() {
    const u32x2 v = *(volatile LAS u32x2*)(size_t)LDS_TAG;
    return ((u64)v[1] << 32) | v[0];
}
__device__ __forceinline__ void lds_wait_word(unsigned a) {
    for (int spin = 0; spin < SPIN_LIMIT && lds_word(a) == 0u; ++spin) __builtin_amdgcn_s_sleep(1);
}
__device__ __forceinline__ void read_epoch(int lane, char* ws, int w) {
    const u64 tag = flag_load(ws, CTL_EPOCH) + 1;
    if (lane == 0) {
        flag_store(ws, CTL_ARR + w, tag);
        *(volatile LAS u32x2*)(size_t)LDS_TAG = (u32x2){(unsigned)tag, (unsigned)(tag >> 32)};
        lds_set(LDS_CNT, 0u); lds_set(LDS_RDY_KV, 0u); lds_set(LDS_RDY_O, 0u);
    }
    wait_lgkm0();
}

static constexpr unsigned STG = 49152, STG_N = 32768;
static constexpr unsigned POISON = 0xffffffffu;

__device__ __forceinline__ void t64_nt(f32x32& acc0, f32x32& acc1, unsigned& am0, unsigned& am1, unsigned& an0, unsigned& an1,
                                       int& slot, int n) {
    int dl;
    asm volatile(ASM_T64_R3
                 : "={v[0:31]}"(acc0), "={v[32:63]}"(acc1),
                   [am0] "+v"(am0), [am1] "+v"(am1), [an0] "+v"(an0), [an1] "+v"(an1),
                   [slot] "+s"(slot), [n] "+s"(n), [dl] "=&s"(dl)
                 : [dwrap] "s"(-2 * (int)STG), [dstep] "s"((int)STG)
                 : "v64", "v65", "v66", "v67", "v68", "v69", "v70", "v71", "v72", "v73", "v74", "v75",
                   "v76", "v77", "v78", "v79", "v80", "v81", "v82", "v83", "v84", "v85", "v86", "v87",
                   "v88", "v89", "v90", "v91", "v92", "v93", "v94", "v95",
                   "v96", "v97", "v98", "v99", "v100", "v101", "v102", "v103", "v104", "v105", "v106", "v107",
                   "v108", "v109", "v110", "v111", "v112", "v113", "v114", "v115", "v116", "v117", "v118", "v119",
                   "v120", "v121", "v122", "v123", "v124", "v125", "v126", "v127", "scc", "memory");
}

__device__ __forceinline__ u32x4 pack8(const f32x32& a, int r, unsigned sign) {
    return (u32x4){pack2(a[r + 0], a[r + 1]) ^ sign, pack2(a[r + 2], a[r + 3]) ^ sign,
                   pack2(a[r + 4], a[r + 5]) ^ sign, pack2(a[r + 6], a[r + 7]) ^ sign};
}

__device__ __forceinline__ void spectral_consumer(int cw, int lane, char* ws, char* out, int w, int wb, int eb, int sb, int tb, int h) {
    const int wm = cw >> 1, wn = cw & 1;
    if (cw == 0) read_epoch(lane, ws, w);
    auto poison_inbox = [&]() {
        char* inbox = ws + WS_X + (size_t)wb * 65536 + (size_t)cw * 8192;
        const u32x4 pv = {POISON, POISON, POISON, POISON};
#pragma unroll
        for (int q = 0; q < (XBF16 ? 4 : 8); ++q) store16_dev(inbox, (unsigned)lane * 16u + q * 1024u, pv);
    };
    if (PDELAY >= 0) {
        if (PDELAY) __builtin_amdgcn_s_sleep(PDELAY < 0 ? 0 : PDELAY);
        poison_inbox();
    }
    const unsigned pos = (unsigned)(((lane >> 4) ^ ((lane & 15) >> 1)) * 16);
    unsigned am0 = (unsigned)((64 * wm + (lane & 15)) * 128) + pos;
    unsigned an0 = STG_N + (unsigned)((64 * wn + (lane & 15)) * 128) + pos;
    unsigned am1 = am0 ^ 64u, an1 = an0 ^ 64u;
    f32x32 acc0, acc1;
    int slot = 0;
    wg_barrier();
    if (CODE_BYTES > 0 && cw == 3) wait_vm<0>();
    if (PDELAY < 0) poison_inbox();
    t64_nt(acc0, acc1, am0, am1, an0, an1, slot, 16);
    lane = fresh(lane);
    {
        constexpr unsigned RS = 128u;
        char* base = ws + WS_ABT + (size_t)((eb * 2 + (wm >> 1)) * 32 + 2 * sb + wn) * 16384 + (size_t)(64 * (wm & 1)) * RS;
        const unsigned sign = (wm >> 1) ? 0x80008000u : 0u;
        const unsigned vo = (unsigned)(lane & 7) * RS + (unsigned)((lane >> 3) & 1) * 64u + (unsigned)(lane >> 4) * 16u;
        const int own = 2 * (wm >> 1) + wn;
        const unsigned r = (unsigned)lane & 15u;
        const unsigned nl = (BDIR == 2 && own == 3 ? 3u * STG : (unsigned)((own + 1) % 3) * STG + STG_N) +
                            (64u * (wm & 1) + 16u * ((r >> 2) & 1u) + 4u * (r >> 3) + (r & 3u)) * 128u +
                            (((unsigned)lane >> 4) ^ (2u * (r >> 3) + ((r & 3u) >> 1))) * 16u;
#pragma unroll
        for (int i = 0; i < 4; ++i) {
            u32x4 a = pack8(i < 2 ? acc0 : acc1, (i & 1) * 16, sign), b = pack8(i < 2 ? acc0 : acc1, (i & 1) * 16 + 8, sign);
            if (BDIR == 2 || own < 3) {
                *(LAS u32x4*)(size_t)(nl + (32u * (i >> 1) + 8u * (i & 1)) * 128u + 64u * (i & 1)) = a;
                *(LAS u32x4*)(size_t)(nl + (32u * (i >> 1) + 8u * (i & 1)) * 128u + 64u * ((i & 1) ^ 1)) = b;
            }
            if (BDIR == 2 && own >= 2) continue;
            pair_rows(a, b, (lane & 8) == 0);
            store16_dev(base, vo + i * 16u * RS, a);
            store16_dev(base, vo + (i * 16u + 8u) * RS, b);
        }
        wait_vm<0>();
        wait_lgkm0();
    }
    wg_barrier();
    t64_nt(acc0, acc1, am0, am1, an0, an1, slot, 32);
    lane = fresh(lane);
    {
        char* peer = ws + WS_X + (size_t)(wb ^ 2) * 65536 + (size_t)cw * 8192;
        const char* inbox = ws + WS_X + (size_t)wb * 65536 + (size_t)cw * 8192;
        const unsigned vx = (unsigned)lane * 16u;
#pragma unroll
        for (int q = 0; q < 4; ++q) {
            u32x4 v;
#pragma unroll
            for (int r = 0; r < 4; ++r) v[r] = __builtin_elementwise_min(pack2(acc1[8 * q + 2 * r], acc1[8 * q + 2 * r + 1]), POISON - 1u);
            store16_dev(peer, vx + q * 1024u, v);
        }
        u32x4 r0, r1, r2, r3;
        for (int spin = 0; spin < SPIN_LIMIT; ++spin) {
            asm volatile("global_load_dwordx4 %0, %4, %5 sc1\n\tglobal_load_dwordx4 %1, %4, %5 offset:1024 sc1\n\t"
                         "global_load_dwordx4 %2, %4, %5 offset:2048 sc1\n\tglobal_load_dwordx4 %3, %4, %5 offset:3072 sc1\n\t"
                         "s_waitcnt vmcnt(0)"
                         : "=&v"(r0), "=&v"(r1), "=&v"(r2), "=&v"(r3)
                         : "v"(vx), "s"(inbox) : "memory");
            auto mx = [](u32x4 a) { return __builtin_elementwise_max(__builtin_elementwise_max(a[0], a[1]), __builtin_elementwise_max(a[2], a[3])); };
            const unsigned m = __builtin_elementwise_max(__builtin_elementwise_max(mx(r0), mx(r1)), __builtin_elementwise_max(mx(r2), mx(r3)));
            if (__all(m != POISON)) break;
            __builtin_amdgcn_s_sleep(1);
        }
        const u32x4 rr[4] = {r0, r1, r2, r3};
#pragma unroll
        for (int q = 0; q < 4; ++q)
#pragma unroll
            for (int r = 0; r < 4; ++r) {
                acc0[8 * q + 2 * r] += __uint_as_float(rr[q][r] << 16);
                acc0[8 * q + 2 * r + 1] += __uint_as_float(rr[q][r] & 0xffff0000u);
            }
        char* base = out + (size_t)(tb * 256 + 64 * wm + 32 * h) * 4096 + (size_t)(1024 + eb * 128 + 64 * wn) * 2;
        const unsigned vo = (unsigned)(lane & 7) * 4096u + (unsigned)((lane >> 3) & 1) * 64u + (unsigned)(lane >> 4) * 16u;
#pragma unroll
        for (int i = 0; i < 2; ++i) {
            u32x4 a = pack8(acc0, i * 16, 0u), b = pack8(acc0, i * 16 + 8, 0u);
            pair_rows(a, b, (lane & 8) == 0);
            store16(base, vo + i * 16u * 4096u, a);
            store16(base, vo + (i * 16u + 8u) * 4096u, b);
        }
    }
}

__device__ __forceinline__ void spectral_loader(int lw, int lane, const char* Bx, const char* Cr, const char* Ci, const char* Dr,
                                                const char* Di, char* ws, int w, int wb, int eb, int sb, int tb, int h) {
    const unsigned swz = (unsigned)(((lane & 7) ^ (4 * (lw & 1) + (lane >> 4))) * 16);
    const unsigned mrow = (unsigned)(8 * lw + (lane >> 3));
    const unsigned nrow = (unsigned)(8 * (2 * (lw & 1) + (lane >> 5)) + 4 * ((lw >> 1) & 1) + ((lane >> 3) & 3));
    const unsigned ldsw = (unsigned)lw * 1024u;
    const i32x4 sdr = make_srd(Dr + (size_t)eb * 128 * 2048, 128 * 2048), sdi = make_srd(Di + (size_t)eb * 128 * 2048, 128 * 2048);
    const i32x4 sx = make_srd(Bx + (size_t)sb * 128 * 2048, 128 * 2048);
    const i32x4 scr = make_srd(Cr + (size_t)tb * 256 * 4096, 256 * 4096), sci = make_srd(Ci + (size_t)tb * 256 * 4096, 256 * 4096);
    const i32x4 sab = make_srd(ws + WS_ABT + (size_t)eb * 64 * 16384, 64 * 16384);
    constexpr unsigned NRS = 128u;
    auto seq_im = [&](unsigned j) { return j < 4u ? (j & 2u) != 0u : ((0xf540u >> (j >> 1)) & 1u) != 0u; };
    auto seq_block = [&](unsigned j) {
        const unsigned i = (unsigned)(0x7654736251432100ull >> (4u * (j >> 1))) & 15u;
        const unsigned o = i == 0 ? (unsigned)tb : (i - 1 < (unsigned)tb ? i - 1 : i);
        return 2u * (8u * (o >> 2) + 4u * (unsigned)h + (o & 3u)) + (j & 1u);
    };
    const unsigned vm1 = mrow * 2048u + swz, vn1 = nrow * 2048u + swz, vm2 = mrow * 4096u + swz, vn2 = nrow * NRS + swz;
    const unsigned rot = KROTB == 0 ? 0u : (unsigned)((sb & 12) + (KROTB >= 2 ? (eb >> 2) * KROTB : 0));
    auto issue1 = [&](unsigned k0, unsigned lds) {
        const unsigned k = (k0 + rot) & 15u;
#pragma unroll
        for (int i = 0; i < 4; ++i) dma16(vm1, sdr, k * 128u + i * 32u * 2048u, lds + i * 4096u);
#pragma unroll
        for (int i = 0; i < 4; ++i) dma16(vn1, sx, k * 128u + i * 32u * 2048u, lds + STG_N + i * 4096u);
#pragma unroll
        for (int i = 0; i < 4; ++i) dma16(vm1, sdi, k * 128u + i * 32u * 2048u, lds + 16384u + i * 4096u);
    };
    auto issue2m = [&](unsigned k, unsigned lds) {
        const unsigned so = seq_block(k) * 128u;
        const bool im = seq_im(k);
        const i32x4 sc = {im ? sci[0] : scr[0], im ? sci[1] : scr[1], scr[2], scr[3]};
#pragma unroll
        for (int i = 0; i < 8; ++i) dma16(vm2, sc, so + (unsigned)(i ^ h) * 32u * 4096u, lds + i * 4096u);
    };
    auto issue2n = [&](unsigned k, unsigned lds) {
        const unsigned so = (seq_block(k) + (seq_im(k) ? 32u : 0u)) * 16384u;
#pragma unroll
        for (int i = 0; i < 4; ++i) dma16_dev(vn2, sab, so + i * 32u * NRS, lds + STG_N + i * 4096u);
    };
    issue1(0, ldsw);
    issue1(1, ldsw + STG);
    if (EARLY & 1) { wait_vm<12>(); wg_barrier(); }
    issue1(2, ldsw + 2 * STG);
    constexpr int NT0 = TOUCH0 >= 2 && TOUCH0 <= 4 ? 2 : 0, NTC = TOUCHC == 2 ? 4 : 0;
    if (NT0) {
        touch8k(Bx, 4u << 20, (unsigned)wb * 32768u + lw * 8192u, lane);
        touch8k(lw < 2 ? Dr : Di, 2u << 20, (unsigned)wb * 16384u + (lw & 1) * 8192u, lane);
    }
    if (!(EARLY & 1)) wait_vm<24 + NT0>();
    if (!(EARLY & 1)) wg_barrier();
    unsigned lds = ldsw;
    u32x2 tagv = {0u, 0u};
#pragma unroll 1
    for (int k = 0; k < 16; ++k) {

        if (k < 2) wait_vm<12 + NT0>(); else if (k < 14) wait_vm<12>(); else if (k == 14) wait_vm<8 + NTC>();
        if (k == 15) { tagv = *(volatile LAS u32x2*)(size_t)LDS_TAG; wait_lgkm0(); }

        wg_barrier();

        if (k < 13) issue1(k + 3, lds); else issue2m(k - 13, lds);
        if (TOUCHC == 6 && k >= TCK && k < TCK + 8 && (lw >> 1) == (k & 1))
            touch8k((lw & 1) ? Ci : Cr, 8u << 20, (unsigned)wb * 65536u + (unsigned)(k - TCK) * 8192u, lane);
        if (TOUCHC == 7 && k >= TCK && k < TCK + 8 && lw == (k & 3))
            touch8k(Cr, 8u << 20, (unsigned)wb * 65536u + (unsigned)(k - TCK) * 8192u, lane);
        if (NTC && k == 13) {
#pragma unroll
            for (int j = 0; j < 4; ++j) touch8k(lw < 2 ? Cr : Ci, 8u << 20, (unsigned)wb * 65536u + ((lw & 1) * 4 + j) * 8192u, lane);
        }
        lds = lds >= 2 * STG ? lds - 2 * STG : lds + STG;

    }
    wait_vm<16>();
    wg_barrier();
    const u64 tag = ((u64)tagv[1] << 32) | tagv[0];
    if (lw == 0 && lane == 0) flag_store(ws, CTL_FA + eb * 16 + sb, tag);
    u32x4 b0[4], b1[4];
    const unsigned own = ldsw + (unsigned)lane * 16u;
#pragma unroll
    for (int i = 0; i < 4; ++i) {
        b0[i] = *(LAS u32x4*)(size_t)(own + STG_N + i * 4096u);
        b1[i] = *(LAS u32x4*)(size_t)(own + 3u * STG + i * 4096u);
    }
    char* bt = ws + WS_ABT + (size_t)(eb * 64 + 32 + 2 * sb) * 16384;
    const u64 fla = (u64)(ws + WS_CTL + (size_t)(CTL_FA + eb * 16) * 128);
    const u64 fl = ((u64)(unsigned)uni((int)(fla >> 32)) << 32) | (unsigned)uni((int)fla);
    const unsigned fo = (unsigned)((4 * h + (lane & 4) * 2 + (lane & 3)) * 128);
    const unsigned fob = (lane & 8) ? (unsigned)((sb ^ 4) * 128) : fo + (CTL_FB - CTL_FA) * 128u;
    u32x2 pv;
    auto poll = [&](unsigned f) {
        pv = ~tagv;
        asm volatile("global_load_dwordx2 %0, %1, %2 sc1" : "+v"(pv) : "v"(f), "s"(fl) : "memory");
    };
    auto spin_on = [&](unsigned f) {
        for (int spin = 0; spin < SPIN_LIMIT && !__all((((u64)pv[1] << 32) | pv[0]) == tag); ++spin) {
            __builtin_amdgcn_s_sleep(1);
            asm volatile("global_load_dwordx2 %0, %1, %2 sc1\n\ts_waitcnt vmcnt(0)" : "=&v"(pv) : "v"(f), "s"(fl) : "memory");
        }
    };
    auto next = [&]() { lds = lds >= 2 * STG ? lds - 2 * STG : lds + STG; };
    wait_vm<8>();
    wg_barrier();
#pragma unroll
    for (int i = 0; i < 4; ++i) *(LAS u32x4*)(size_t)(lds + STG_N + (unsigned)lane * 16u + i * 4096u) = b1[i];
    poll(fo);
    issue2m(3, lds);
    next();
    if (lw == 0) wait_vm<10>(); else wait_vm<9>();
    wait_lgkm0();
    wg_barrier();
    issue2m(4, lds);
    asm volatile("s_waitcnt vmcnt(16)" : "+v"(pv) :: "memory");
    spin_on(fo);
    issue2n(4, lds);
    next();
    wait_vm<12>();
    wg_barrier();
    issue2n(5, lds); issue2m(5, lds);
#pragma unroll
    for (int i = 0; i < 4; ++i) store16_dev(bt, vn2 + i * 4096u, b0[i]);
    next();
    wait_vm<16>();
    wg_barrier();
    issue2n(6, lds); issue2m(6, lds);
#pragma unroll
    for (int i = 0; i < 4; ++i) store16_dev(bt + 16384, vn2 + i * 4096u, b1[i]);
    next();
    constexpr int KB = BORD == 2 ? 8 : 13;
#pragma unroll 1
    for (int k = 4; k < KB; ++k) {
        if (k == 4) wait_vm<20>(); else if (k == 5) wait_vm<16>(); else if (k == 7 && lw == 0) wait_vm<13>();
        else wait_vm<12>();
        wg_barrier();
        if (k == 6 && lw == 0 && lane == 0) flag_store(ws, CTL_FB + eb * 16 + sb, tag);
        issue2n(k + 3, lds); issue2m(k + 3, lds);
        next();
    }
    wait_vm<12>();
    wg_barrier();
    poll(fob);
    issue2n(KB + 3, lds); issue2m(KB + 3, lds);
    next();
    asm volatile("s_waitcnt vmcnt(%1)" : "+v"(pv) : "n"(12) : "memory");
    wg_barrier();
    spin_on(fob);
    issue2n(KB + 4, lds); issue2m(KB + 4, lds);
    next();
#pragma unroll 1
    for (int k = KB + 2; k < 32; ++k) {
        if (k < 30) wait_vm<12>(); else if (k == 30) wait_vm<0>();
        wg_barrier();
        if (k < 29) { issue2n(k + 3, lds); issue2m(k + 3, lds); }
        next();
    }
    if (w == 0 && lw == 3) {
        for (int spin = 0; spin < SPIN_LIMIT; ++spin) {
            bool ok = true;
#pragma unroll
            for (int q = 0; q < 4; ++q) ok = ok && (flag_load(ws, CTL_ARR + q * 64 + lane) == tag);
            if (__all(ok)) break;
            __builtin_amdgcn_s_sleep(4);
        }
        if (lane == 0) flag_store(ws, CTL_EPOCH, tag);
    }
}

static constexpr unsigned LDS_NRING = 96 * 1024;
static constexpr unsigned LDS_PREV = 64 * 1024;
static constexpr float Q_SCALE = 0.17677669529663687f * 1.4426950408889634f;

__device__ __forceinline__ f32x4 mfma16(bf16x8 a, bf16x8 b, f32x4 c) { return __builtin_amdgcn_mfma_f32_16x16x32_bf16(a, b, c, 0, 0, 0); }
__device__ __forceinline__ bf16x8 lds_read16(unsigned a) { return *(LAS bf16x8*)(size_t)a; }
__device__ __forceinline__ bf16x4 lds_read_tr(unsigned a) { return __builtin_amdgcn_ds_read_tr16_b64_v4bf16((LAS bf16x4*)(size_t)a); }
__device__ __forceinline__ void lds_write16(unsigned a, u32x4 v) { *(LAS u32x4*)(size_t)a = v; }
__device__ __forceinline__ void swap16(unsigned& a, unsigned& b) {
    auto r = __builtin_amdgcn_permlane16_swap(a, b, false, false);
    a = r[0]; b = r[1];
}
__device__ __forceinline__ float row_max(float x) {
    auto r = __builtin_amdgcn_permlane32_swap(__float_as_uint(x), __float_as_uint(x), false, false);
    x = __builtin_fmaxf(__uint_as_float(r[0]), __uint_as_float(r[1]));
    auto t = __builtin_amdgcn_permlane16_swap(__float_as_uint(x), __float_as_uint(x), false, false);
    return __builtin_fmaxf(__uint_as_float(t[0]), __uint_as_float(t[1]));
}
__device__ __forceinline__ void row_max4(float (&x)[4]) {
    float t0, t1, t2, t3;
    asm volatile("v_mov_b32 %4, %0\n\tv_mov_b32 %5, %1\n\tv_mov_b32 %6, %2\n\tv_mov_b32 %7, %3\n\t"
                 "v_permlane32_swap_b32 %0, %4\n\tv_permlane32_swap_b32 %1, %5\n\t"
                 "v_permlane32_swap_b32 %2, %6\n\tv_permlane32_swap_b32 %3, %7\n\t"
                 "v_max_f32 %0, %0, %4\n\tv_max_f32 %1, %1, %5\n\tv_max_f32 %2, %2, %6\n\tv_max_f32 %3, %3, %7\n\t"
                 "v_mov_b32 %4, %0\n\tv_mov_b32 %5, %1\n\tv_mov_b32 %6, %2\n\tv_mov_b32 %7, %3\n\t"
                 "v_permlane16_swap_b32 %0, %4\n\tv_permlane16_swap_b32 %1, %5\n\t"
                 "v_permlane16_swap_b32 %2, %6\n\tv_permlane16_swap_b32 %3, %7\n\t"
                 "v_max_f32 %0, %0, %4\n\tv_max_f32 %1, %1, %5\n\tv_max_f32 %2, %2, %6\n\tv_max_f32 %3, %3, %7\n\ts_nop 1"
                 : "+v"(x[0]), "+v"(x[1]), "+v"(x[2]), "+v"(x[3]), "=&v"(t0), "=&v"(t1), "=&v"(t2), "=&v"(t3));
}
__device__ __forceinline__ float max4(float m, f32x4 s) {
    return __builtin_fmaxf(__builtin_fmaxf(__builtin_fmaxf(__builtin_fmaxf(m, s[0]), s[1]), s[2]), s[3]);
}
__device__ __forceinline__ bf16x8 as_frag(u32x4 v) { return __builtin_bit_cast(bf16x8, v); }

__device__ __forceinline__ void qkv_gemm(f32x32& a0, f32x32& a1, f32x32& a2, unsigned am0, unsigned am1, unsigned an0, unsigned an1) {
    int ms = 0, ns = 0, n = 16, dm, dn;
    asm volatile(ASM_QKV
                 : "={v[0:31]}"(a0), "={v[32:63]}"(a1), "={v[64:95]}"(a2),
                   [am0] "+v"(am0), [am1] "+v"(am1), [an0] "+v"(an0), [an1] "+v"(an1),
                   [ms] "+s"(ms), [ns] "+s"(ns), [n] "+s"(n), [dm] "=&s"(dm), [dn] "=&s"(dn)
                 : [mwrap] "s"(-65536), [mstep] "s"(32768), [nwrap] "s"(-24576), [nstep] "s"(24576)
                 : "v96", "v97", "v98", "v99", "v100", "v101", "v102", "v103", "v104", "v105", "v106", "v107",
                   "v108", "v109", "v110", "v111", "v112", "v113", "v114", "v115", "v116", "v117", "v118", "v119",
                   "v120", "v121", "v122", "v123", "v124", "v125", "v126", "v127", "v128", "v129", "v130", "v131",
                   "v132", "v133", "v134", "v135", "v136", "v137", "v138", "v139", "v140", "v141", "v142", "v143",
                   "v144", "v145", "v146", "v147", "v148", "v149", "v150", "v151", "scc", "memory");
}

struct AttnState {
    bf16x8 q[4];
    f32x4 o[4][2];
    f32x4 l[4];
    float m[4];
};

template <int MODE0, int MODE1>
__device__ __forceinline__ void max_pair(const AttnState& st, float (&mx)[4], int qb, bf16x8 k0, bf16x8 k1, f32x4 bias) {
    const f32x4 zero = {0.f, 0.f, 0.f, 0.f};
    if (MODE0) mx[qb] = max4(mx[qb], mfma16(k0, st.q[qb], MODE0 == 2 ? bias : zero));
    if (MODE1) mx[qb] = max4(mx[qb], mfma16(k1, st.q[qb], MODE1 == 2 ? bias : zero));
}
template <bool PREV>
__device__ __forceinline__ void attn_max(const AttnState& st, float (&mx)[4], unsigned ka, int wm, f32x4 bias) {
    unsigned a = PREV ? ka + (unsigned)wm * 2048u : ka;
    if (!PREV) {
#pragma unroll 1
        for (int it = 0; it < wm; ++it, a += 2048u) {
            const bf16x8 k0 = lds_read16(a), k1 = lds_read16(a + 1024u);
#pragma unroll
            for (int qb = 0; qb < 4; ++qb) max_pair<1, 1>(st, mx, qb, k0, k1, bias);
        }
    }
    {
        const bf16x8 k0 = lds_read16(a), k1 = lds_read16(a + 1024u);
        if (PREV) {
            max_pair<2, 1>(st, mx, 0, k0, k1, bias);
            max_pair<0, 2>(st, mx, 1, k0, k1, bias);
        } else {
            max_pair<2, 0>(st, mx, 0, k0, k1, bias);
            max_pair<1, 2>(st, mx, 1, k0, k1, bias);
            max_pair<1, 1>(st, mx, 2, k0, k1, bias);
            max_pair<1, 1>(st, mx, 3, k0, k1, bias);
        }
        a += 2048u;
    }
#pragma unroll 1
    for (int it = 2 * wm; it < 6; it += 2, a += 4096u) {
        const bf16x8 k0 = lds_read16(a), k1 = lds_read16(a + 1024u), k2 = lds_read16(a + 2048u), k3 = lds_read16(a + 3072u);
        max_pair<1, 1>(st, mx, PREV ? 0 : 2, k0, k1, bias);
        max_pair<1, 1>(st, mx, PREV ? 1 : 3, k0, k1, bias);
        max_pair<1, 1>(st, mx, PREV ? 0 : 2, k2, k3, bias);
        max_pair<1, 1>(st, mx, PREV ? 1 : 3, k2, k3, bias);
    }
    {
        const bf16x8 k0 = lds_read16(a), k1 = lds_read16(a + 1024u);
        if (PREV) {
            max_pair<1, 1>(st, mx, 0, k0, k1, bias);
            max_pair<1, 1>(st, mx, 1, k0, k1, bias);
            max_pair<2, 1>(st, mx, 2, k0, k1, bias);
            max_pair<0, 2>(st, mx, 3, k0, k1, bias);
        } else {
            max_pair<2, 0>(st, mx, 2, k0, k1, bias);
            max_pair<1, 2>(st, mx, 3, k0, k1, bias);
        }
        a += 2048u;
    }
    if (PREV) {
#pragma unroll 1
        for (int it = 0; it < wm; ++it, a += 2048u) {
            const bf16x8 k0 = lds_read16(a), k1 = lds_read16(a + 1024u);
#pragma unroll
            for (int qb = 0; qb < 4; ++qb) max_pair<1, 1>(st, mx, qb, k0, k1, bias);
        }
    }
}

__device__ __forceinline__ u32x2 exp_pack(f32x4 s) {
    u32x2 r;
    r[0] = pack2(__builtin_amdgcn_exp2f(s[0]), __builtin_amdgcn_exp2f(s[1]));
    r[1] = pack2(__builtin_amdgcn_exp2f(s[2]), __builtin_amdgcn_exp2f(s[3]));
    return r;
}
struct VFrag { bf16x8 d0, d1; };
__device__ __forceinline__ VFrag read_v(unsigned va0, unsigned va1, unsigned off) {
    VFrag v;
    const bf16x4 a = lds_read_tr(va0 + off), b = lds_read_tr(va0 + off + 1024u);
    const bf16x4 c = lds_read_tr(va1 + off), d = lds_read_tr(va1 + off + 1024u);
    v.d0 = __builtin_shufflevector(a, b, 0, 1, 2, 3, 4, 5, 6, 7);
    v.d1 = __builtin_shufflevector(c, d, 0, 1, 2, 3, 4, 5, 6, 7);
    return v;
}
template <int MODE0, int MODE1, bool FIRST = false>
__device__ __forceinline__ void attn_pair(AttnState& st, int qb, bf16x8 k0, bf16x8 k1, const VFrag& v, f32x4 bias, bf16x8 ones) {
    const float nm = -st.m[qb];
    const f32x4 c = {nm, nm, nm, nm};
    u32x2 p0 = {0u, 0u}, p1 = {0u, 0u};
    if (MODE0) p0 = exp_pack(mfma16(k0, st.q[qb], MODE0 == 2 ? c + bias : c));
    if (MODE1) p1 = exp_pack(mfma16(k1, st.q[qb], MODE1 == 2 ? c + bias : c));
    const u32x4 pp = {p0[0], p0[1], p1[0], p1[1]};
    const bf16x8 p = as_frag(pp);
    const f32x4 zero = {0.f, 0.f, 0.f, 0.f};
    st.o[qb][0] = mfma16(v.d0, p, FIRST ? zero : st.o[qb][0]);
    st.o[qb][1] = mfma16(v.d1, p, FIRST ? zero : st.o[qb][1]);
    st.l[qb] = mfma16(ones, p, FIRST ? zero : st.l[qb]);
}
template <bool PREV>
__device__ __forceinline__ void attn_acc(AttnState& st, unsigned ka, unsigned va0, unsigned va1, int wm, f32x4 bias, bf16x8 ones) {
    unsigned off = PREV ? (unsigned)wm * 2048u : 0u;
    if (ATT2 && !PREV) {
        const unsigned od = (unsigned)wm * 2048u;
        const bf16x8 k0 = lds_read16(ka + od), k1 = lds_read16(ka + od + 1024u);
        const VFrag v = read_v(va0, va1, od);
        attn_pair<1, 1, true>(st, 2, k0, k1, v, bias, ones);
        attn_pair<1, 1, true>(st, 3, k0, k1, v, bias, ones);
        attn_pair<2, 0, true>(st, 0, k0, k1, v, bias, ones);
        attn_pair<1, 2, true>(st, 1, k0, k1, v, bias, ones);
    }
    if (!PREV) {
#pragma unroll 1
        for (int it = 0; it < wm; ++it, off += 2048u) {
            const bf16x8 k0 = lds_read16(ka + off), k1 = lds_read16(ka + off + 1024u);
            const VFrag v = read_v(va0, va1, off);
#pragma unroll
            for (int qb = 0; qb < 4; ++qb) attn_pair<1, 1>(st, qb, k0, k1, v, bias, ones);
        }
    }
    if (ATT2 && !PREV) {
        off += 2048u;
    } else {
        const bf16x8 k0 = lds_read16(ka + off), k1 = lds_read16(ka + off + 1024u);
        const VFrag v = read_v(va0, va1, off);
        if (PREV) {
            attn_pair<2, 1>(st, 0, k0, k1, v, bias, ones);
            attn_pair<0, 2>(st, 1, k0, k1, v, bias, ones);
        } else {
            attn_pair<2, 0>(st, 0, k0, k1, v, bias, ones);
            attn_pair<1, 2>(st, 1, k0, k1, v, bias, ones);
            attn_pair<1, 1>(st, 2, k0, k1, v, bias, ones);
            attn_pair<1, 1>(st, 3, k0, k1, v, bias, ones);
        }
        off += 2048u;
    }
    if (APRIO & 2) __builtin_amdgcn_s_setprio(0);
#pragma unroll 1
    for (int it = 2 * wm; it < 6; it += 2, off += 4096u) {
        const bf16x8 k0 = lds_read16(ka + off), k1 = lds_read16(ka + off + 1024u);
        const bf16x8 k2 = lds_read16(ka + off + 2048u), k3 = lds_read16(ka + off + 3072u);
        const VFrag v = read_v(va0, va1, off), u = read_v(va0, va1, off + 2048u);
        attn_pair<1, 1>(st, PREV ? 0 : 2, k0, k1, v, bias, ones);
        attn_pair<1, 1>(st, PREV ? 1 : 3, k0, k1, v, bias, ones);
        attn_pair<1, 1>(st, PREV ? 0 : 2, k2, k3, u, bias, ones);
        attn_pair<1, 1>(st, PREV ? 1 : 3, k2, k3, u, bias, ones);
    }
    {
        const bf16x8 k0 = lds_read16(ka + off), k1 = lds_read16(ka + off + 1024u);
        const VFrag v = read_v(va0, va1, off);
        if (PREV) {
            attn_pair<1, 1>(st, 0, k0, k1, v, bias, ones);
            attn_pair<1, 1>(st, 1, k0, k1, v, bias, ones);
            attn_pair<2, 1>(st, 2, k0, k1, v, bias, ones);
            attn_pair<0, 2>(st, 3, k0, k1, v, bias, ones);
        } else {
            attn_pair<2, 0>(st, 2, k0, k1, v, bias, ones);
            attn_pair<1, 2>(st, 3, k0, k1, v, bias, ones);
        }
        off += 2048u;
    }
    if (PREV) {
#pragma unroll 1
        for (int it = 0; it < wm; ++it, off += 2048u) {
            const bf16x8 k0 = lds_read16(ka + off), k1 = lds_read16(ka + off + 1024u);
            const VFrag v = read_v(va0, va1, off);
#pragma unroll
            for (int qb = 0; qb < 4; ++qb) attn_pair<1, 1>(st, qb, k0, k1, v, bias, ones);
        }
    }
}

__device__ __forceinline__ void t64_tr(f32x32& acc0, f32x32& acc1, i32x16& fm, i32x16& fn, unsigned am0, unsigned am1,
                                       unsigned an0, unsigned an1, unsigned an2, unsigned an3) {
    int dl, slot = 0, n = 16;
    asm volatile(ASM_T64_TR
                 : "={v[0:31]}"(acc0), "={v[32:63]}"(acc1), "+{v[64:79]}"(fm), "+{v[80:95]}"(fn),
                   [am0] "+v"(am0), [am1] "+v"(am1), [an0] "+v"(an0), [an1] "+v"(an1), [an2] "+v"(an2), [an3] "+v"(an3),
                   [slot] "+s"(slot), [n] "+s"(n), [dl] "=&s"(dl)
                 : [z] "s"(1), [dwrap] "s"(-(ORING - 1) * 32768), [dstep] "s"(32768), [nring] "s"(ORING)
                 : "v96", "v97", "v98", "v99", "v100", "v101", "v102", "v103", "v104", "v105", "v106", "v107",
                   "v108", "v109", "v110", "v111", "v112", "v113", "v114", "v115", "v116", "v117", "v118", "v119",
                   "v120", "v121", "v122", "v123", "v124", "v125", "v126", "v127", "scc", "memory");
}

template <typename F>
__device__ __forceinline__ void oproj_stream(F issue) {
    wait_vm<12>();
    wg_barrier();
    if (ORING == 5) issue(4);
#pragma unroll 1
    for (int k = 0; k < 16; ++k) {
        if (k < 12) wait_vm<4 * (ORING - 2)>(); else if (k == 12) wait_vm<8>(); else if (k == 13) wait_vm<4>(); else wait_vm<0>();
        wg_barrier();
        if (k + ORING < 16) issue(k + ORING);
    }
}
__device__ __forceinline__ void oproj_weights(int lw, int lane, const char* Wo, int p) {
    const int r = lane >> 4, P = lane & 15;
    const unsigned c = (unsigned)((P >> 1) ^ (r | (((lw >> 1) & 1) << 2)));
    const unsigned vn = (unsigned)(16 * (lw & 1) + 4 * (lw >> 1) + r) * 2048u + c * 32u + (unsigned)(P & 1) * 16u;
    const i32x4 srdn = make_srd(Wo + (size_t)(p >> 1) * 256, 1024 * 2048 - (p >> 1) * 256);
    auto issue_n = [&](int k) {
        const unsigned lds = (unsigned)(k % ORING) * 32768u + 16384u + (unsigned)lw * 1024u;
#pragma unroll
        for (int i = 0; i < 4; ++i)
            dma16(vn, srdn, ((unsigned)k * 64u + 32u * (i >> 1) + 8u * (i & 1)) * 2048u, lds + i * 4096u);
    };
#pragma unroll
    for (int k = 0; k < 4; ++k) issue_n(k);
    oproj_stream(issue_n);
}

__device__ __forceinline__ void attn_consumer(int cw, int lane, char* ws, char* out, const char* Wo, const char* Cr, const char* Ci, int w, int wa, int ib, int p) {
    const int wn = cw & 1, wm = cw < 4 ? (cw >> 1) : 3 - ((cw - 4) >> 1);
    if (cw == 0) read_epoch(lane, ws, w);
    AttnState st;
    int lr = lane & 15, g = lane >> 4, q4 = (lane >> 2) & 3, p4 = lane & 3;
    unsigned koff;
    {
        const unsigned am0 = (unsigned)((64 * wm + lr) * 128 + ((g ^ (lr >> 1)) * 16));
        const unsigned nsw = (unsigned)(((q4 >> 1) & 1) | ((g & 1) << 1));
        const unsigned anb = LDS_NRING + (unsigned)((8 * g + q4) * 128 + p4 * 8);
        const unsigned an0 = anb + (((unsigned)(2 * wn) ^ nsw) * 32u), an1 = anb + (((unsigned)(2 * wn + 1) ^ nsw) * 32u);
        f32x32 a0, a1, a2;
        wg_barrier();
        if (TOUCHC == 4) {
            touch8k(Cr, 8u << 20, (unsigned)wa * 65536u + cw * 8192u, lane);
            touch8k(Ci, 8u << 20, (unsigned)wa * 65536u + cw * 8192u, lane);
        }
        qkv_gemm(a0, a1, a2, am0, am0 ^ 64u, an0, an1);
        lane = fresh(lane);
        lr = lane & 15; g = lane >> 4; q4 = (lane >> 2) & 3; p4 = lane & 3;
        koff = (unsigned)(lr * 64 + ((g ^ ((-(lr >> 2)) & 3)) * 16));
#define ACC(i, j, r) ((24 * (i) + 4 * (j) + (r)) < 32 ? a0[(24 * (i) + 4 * (j) + (r)) & 31] : (24 * (i) + 4 * (j) + (r)) < 64 ? a1[(24 * (i) + 4 * (j) + (r)) & 31] : a2[(24 * (i) + 4 * (j) + (r)) & 31])
        char* img = ws + WS_KV + (size_t)(ib * 16 + p) * 65536;
        const unsigned kw = (unsigned)(wn * 16384) + koff;
        const unsigned vw = (unsigned)(32768 + wn * 16384 + lr * 64 + ((((g & 1) ^ ((lr >> 2) & 1)) * 2 + (g >> 1)) * 16));
#pragma unroll
        for (int i = 0; i < 4; ++i) {
            u32x4 k = {pack2(ACC(i, 2, 0), ACC(i, 2, 1)), pack2(ACC(i, 2, 2), ACC(i, 2, 3)),
                       pack2(ACC(i, 3, 0), ACC(i, 3, 1)), pack2(ACC(i, 3, 2), ACC(i, 3, 3))};
            const unsigned blk = (unsigned)(i < 2 ? 2 * wm + i : 12 - 2 * wm + i) * 1024u;
            lds_write16(kw + blk, k);
            if (KVDIR && ib < 7) store16_dev(img, kw + blk, k);
            unsigned x0 = pack2(ACC(i, 4, 0), ACC(i, 4, 1)), x1 = pack2(ACC(i, 4, 2), ACC(i, 4, 3));
            unsigned y0 = pack2(ACC(i, 5, 0), ACC(i, 5, 1)), y1 = pack2(ACC(i, 5, 2), ACC(i, 5, 3));
            swap16(x0, y0); swap16(x1, y1);
            lds_write16(vw + blk, (u32x4){x0, x1, y0, y1});
            if (KVDIR && ib < 7) store16_dev(img, vw + blk, (u32x4){x0, x1, y0, y1});
            st.q[i] = as_frag((u32x4){pack2(ACC(i, 0, 0) * Q_SCALE, ACC(i, 0, 1) * Q_SCALE), pack2(ACC(i, 0, 2) * Q_SCALE, ACC(i, 0, 3) * Q_SCALE),
                                      pack2(ACC(i, 1, 0) * Q_SCALE, ACC(i, 1, 1) * Q_SCALE), pack2(ACC(i, 1, 2) * Q_SCALE, ACC(i, 1, 3) * Q_SCALE)});
        }
#undef ACC
    }
    wait_lgkm0();
    wg_barrier();
    const float ninf = -__builtin_inff();
    f32x4 bias_own, bias_prev;
#pragma unroll
    for (int r = 0; r < 4; ++r) {
        const bool le = 4 * g + r <= lr;
        bias_own[r] = le ? 0.f : ninf;
        bias_prev[r] = le ? ninf : 0.f;
    }
    u32x4 ones_u = {0x3f803f80u, 0x3f803f80u, 0x3f803f80u, 0x3f803f80u};
    if (ATT2) asm volatile("" : "+v"(ones_u));
    const bf16x8 ones = as_frag(ones_u);
    const unsigned ka = (unsigned)(wn * 16384) + koff;
    const unsigned vb = (unsigned)(32768 + wn * 16384 + (4 * g + q4) * 64 + p4 * 8);
    const unsigned va0 = vb + (unsigned)((g & 1) * 32), va1 = vb + (unsigned)(((g & 1) ^ 1) * 32);
    {
        float mx[4] = {ninf, ninf, ninf, ninf};
        if ((APRIO & 1) && cw >= 4) __builtin_amdgcn_s_setprio(1);
        attn_max<false>(st, mx, ka, wm, bias_own);
        if (KVDIR && ib < 7) {
            wait_vm<0>();
            if ((fresh(lane) & 63) == 0) {
                const unsigned old = __hip_atomic_fetch_add((LAS unsigned*)(size_t)LDS_CNT, 1u, __ATOMIC_RELAXED, __HIP_MEMORY_SCOPE_WORKGROUP);
                if (old == 7u) flag_store(ws, CTL_KV + ib * 16 + p, lds_tag());
            }
        }
        if (ATT2) {
            row_max4(mx);
#pragma unroll
            for (int qb = 0; qb < 4; ++qb) st.m[qb] = mx[qb];
        } else {
#pragma unroll
            for (int qb = 0; qb < 4; ++qb) {
                st.m[qb] = row_max(mx[qb]);
                st.o[qb][0] = st.o[qb][1] = st.l[qb] = (f32x4){0.f, 0.f, 0.f, 0.f};
            }
        }
        attn_acc<false>(st, ka, va0, va1, wm, bias_own, ones);
        if (APRIO) __builtin_amdgcn_s_setprio(0);
    }
    wg_barrier();
    if (ib > 0) {
        float mx[4] = {st.m[0], st.m[1], st.m[2], st.m[3]};
        if ((APRIO & 1) && cw >= 4) __builtin_amdgcn_s_setprio(1);
        attn_max<true>(st, mx, ka + LDS_PREV, wm, bias_prev);
        if (ATT2) row_max4(mx);
#pragma unroll
        for (int qb = 0; qb < 4; ++qb) {
            const float mn = ATT2 ? mx[qb] : row_max(mx[qb]);
            const float al = __builtin_amdgcn_exp2f(st.m[qb] - mn);
            st.m[qb] = mn;
            st.o[qb][0] *= al; st.o[qb][1] *= al; st.l[qb] *= al;
        }
        attn_acc<true>(st, ka + LDS_PREV, va0 + LDS_PREV, va1 + LDS_PREV, wm, bias_prev, ones);
        if (APRIO) __builtin_amdgcn_s_setprio(0);
    }
    lane = fresh(lane);
    lr = lane & 15; g = lane >> 4; q4 = (lane >> 2) & 3; p4 = lane & 3;
    {
        char* ob = ws + WS_O + (size_t)(ib * 16 + p) * 32768;
#pragma unroll
        for (int qb = 0; qb < 4; ++qb) {
            const float inv = ATT2 ? __builtin_amdgcn_rcpf(st.l[qb][0]) : 1.0f / st.l[qb][0];
            const int row = 16 * (qb < 2 ? 2 * wm + qb : 12 - 2 * wm + qb) + lr;
            u32x4 v = {pack2(st.o[qb][0][0] * inv, st.o[qb][0][1] * inv), pack2(st.o[qb][0][2] * inv, st.o[qb][0][3] * inv),
                       pack2(st.o[qb][1][0] * inv, st.o[qb][1][1] * inv), pack2(st.o[qb][1][2] * inv, st.o[qb][1][3] * inv)};
            store16_dev(ob, (unsigned)(row * 128 + (((4 * wn + g) ^ ((row >> 1) & 7)) * 16)), v);
        }
        wait_vm<0>();
    }
    wg_barrier();
    if (cw >= 4) {
        if (OLD8) oproj_weights(cw - 4, lane, Wo, p);
        return;
    }
    {
        const int om = cw >> 1, on = cw & 1;
        const unsigned am0 = (unsigned)((64 * om + lr) * 128 + ((g ^ (lr >> 1)) * 16));
        const unsigned anb = 16384u + (unsigned)((8 * g + q4) * 256 + p4 * 8);
        const unsigned nsw = (unsigned)(q4 | ((g & 1) << 2));
        const unsigned an0 = anb + (((unsigned)(4 * on + 0) ^ nsw) * 32u), an1 = anb + (((unsigned)(4 * on + 1) ^ nsw) * 32u);
        const unsigned an2 = anb + (((unsigned)(4 * on + 2) ^ nsw) * 32u), an3 = anb + (((unsigned)(4 * on + 3) ^ nsw) * 32u);
        f32x32 acc0, acc1;
        i32x16 fm, fn;
        wg_barrier();
        asm volatile(
            "ds_read_b128 v[64:67], %2\n ds_read_b128 v[68:71], %2 offset:2048\n ds_read_b128 v[72:75], %2 offset:4096\n ds_read_b128 v[76:79], %2 offset:6144\n"
            "ds_read_b64_tr_b16 v[80:81], %3\n ds_read_b64_tr_b16 v[82:83], %3 offset:1024\n"
            "ds_read_b64_tr_b16 v[84:85], %4\n ds_read_b64_tr_b16 v[86:87], %4 offset:1024\n"
            "ds_read_b64_tr_b16 v[88:89], %5\n ds_read_b64_tr_b16 v[90:91], %5 offset:1024\n"
            "ds_read_b64_tr_b16 v[92:93], %6\n ds_read_b64_tr_b16 v[94:95], %6 offset:1024\n"
            "s_waitcnt lgkmcnt(0)\n"
            : "={v[64:79]}"(fm), "={v[80:95]}"(fn) : "v"(am0), "v"(an0), "v"(an1), "v"(an2), "v"(an3) : "memory");
        t64_tr(acc0, acc1, fm, fn, am0, am0 ^ 64u, an0, an1, an2, an3);
        char* ob = out + (size_t)(256 * ib + 128 * (p & 1) + 64 * om) * 4096 + (size_t)(128 * (p >> 1) + 64 * on) * 2;
        const unsigned vo = FULLLINE ? (unsigned)((lr & 7) * 4096 + (lr >> 3) * 64 + ((g & 1) * 2 + (g >> 1)) * 16)
                                     : (unsigned)(lr * 4096 + ((g & 1) * 2 + (g >> 1)) * 16);
#pragma unroll
        for (int i = 0; i < 4; ++i) {
            u32x4 pc[2];
#pragma unroll
            for (int J = 0; J < 2; ++J) {
                const int r = (i & 1) * 16 + J * 8;
                const f32x32& a = (i < 2) ? acc0 : acc1;
                unsigned x0 = pack2(a[r + 0], a[r + 1]), x1 = pack2(a[r + 2], a[r + 3]);
                unsigned y0 = pack2(a[r + 4], a[r + 5]), y1 = pack2(a[r + 6], a[r + 7]);
                swap16(x0, y0); swap16(x1, y1);
                pc[J] = (u32x4){x0, x1, y0, y1};
            }
            if (FULLLINE) {
                pair_rows(pc[0], pc[1], lr < 8);
                store16(ob, vo + i * 16u * 4096u, pc[0]);
                store16(ob, vo + (i * 16u + 8u) * 4096u, pc[1]);
            } else {
                store16(ob, vo + i * 16u * 4096u, pc[0]);
                store16(ob, vo + i * 16u * 4096u + 64u, pc[1]);
            }
        }
    }
}

__device__ __forceinline__ void attn_loader(int lw, int lane, const char* Ax, const char* Wqkv, const char* Wo, const char* Cr, const char* Ci,
                                            char* ws, int wa, int ib, int p) {
    {
        const unsigned swz = (unsigned)(((lane & 7) ^ (4 * (lw & 1) + (lane >> 4))) * 16);
        const unsigned vm = (unsigned)(8 * lw + (lane >> 3)) * 2048u + swz;
        const int r8 = lane >> 3, P = lane & 7;
        const unsigned c = (unsigned)((P >> 1) ^ (((r8 >> 1) & 1) | ((lw & 1) << 1)));
        const unsigned vn = (unsigned)(8 * lw + r8) * 6144u + c * 32u + (unsigned)(P & 1) * 16u;
        const i32x4 srdm = make_srd(Ax + (size_t)ib * 256 * 2048, 256 * 2048);
        const i32x4 srdn = make_srd(Wqkv + (size_t)p * 128, 1024 * 6144 - p * 128);
        const unsigned ldsw = (unsigned)lw * 1024u;
        const unsigned rot = KROT == 0 ? 0u : (unsigned)((p & 12) + (KROT >= 2 ? (ib >> 2) * KROT : 0));
        auto issue_m = [&](int k) {
            const unsigned lds = (unsigned)(k % 3) * 32768u + ldsw, kk = ((unsigned)k + rot) & 15u;
#pragma unroll
            for (int i = 0; i < 8; ++i)
                dma16(vm, srdm, kk * 128u + (unsigned)((i & 1) ? 7 - (i >> 1) : (i >> 1)) * 32u * 2048u, lds + i * 4096u);
        };
        auto issue_n = [&](int k) {
            const unsigned lds = LDS_NRING + (unsigned)(k & 1) * 24576u + ldsw, kk = ((unsigned)k + rot) & 15u;
#pragma unroll
            for (int s = 0; s < 3; ++s)
#pragma unroll
                for (int i = 0; i < 2; ++i)
                    dma16(vn, srdn, kk * 64u * 6144u + i * 32u * 6144u + s * 2048u, lds + s * 8192u + i * 4096u);
        };
        const unsigned pfj = (unsigned)((ib & 3) * 48 + lw * 12 + lane);
        const unsigned pfn = (unsigned)(((ib & 3) + 4 * (p & 3)) * 48 + lw * 12 + lane), pfr = pfn >> 2;
        const unsigned vpf = lane >= 12 ? 0x7ff00000u
                           : NMIX ? (pfr / 3u) * 6144u + (pfr % 3u) * 2048u + (pfn & 3u) * 128u : (pfj / 3u) * 6144u + (pfj % 3u) * 2048u;
        const i32x4 srdp = NMIX ? make_srd(Wqkv + (size_t)(p & ~3) * 128, 1024 * 6144 - (p & ~3) * 128) : srdn;
        const GAS char* pfa = lane < 12 ? (const GAS char*)Wqkv + (NMIX ? p & ~3 : p) * 128 + vpf
                                        : (const GAS char*)Ax + (size_t)(ib * 256 + (p & 3) * 64 + lw * 16 + ((lane - 12) & 15)) * 2048;
        const unsigned pfi = lane < 12 ? 64u * 6144u : 128u;
        auto prefetch_n = [&](int k) {
            if (MPF) {
                const GAS char* a = pfa + (size_t)((((unsigned)k + rot) & 15u) * pfi);
                if (lane < 28) asm volatile("s_mov_b32 m0, %1\n\ts_nop 0\n\tglobal_load_lds_dword %0, off" :: "v"(a), "s"(LDS_DUMMY) : "memory");
            } else {
                asm volatile("s_mov_b32 m0, %3\n\ts_nop 0\n\tbuffer_load_dword %0, %1, %2 offen lds"
                             :: "v"(vpf), "s"(srdp), "s"((((unsigned)k + rot) & 15u) * 64u * 6144u), "s"(LDS_DUMMY) : "memory");
            }
        };
        issue_n(0); issue_m(0);
        issue_n(1);
        if (EARLY & 4) { wait_vm<6>(); wg_barrier(); }
        issue_m(1);
        if (EARLY & 2) { wait_vm<14>(); wg_barrier(); }
        issue_m(2);
        constexpr int NT0 = (TOUCH0 == 2 ? 3 : (TOUCH0 == 3 || TOUCH0 == 5) ? 1 : 0) + (NPF ? NPF - 2 : 0), NTA = TOUCHC == 3 ? 4 + TOUCHW : 0;
        if (NPF) {
#pragma unroll
            for (int k = 2; k < NPF; ++k) prefetch_n(k);
        }
        if (TOUCH0 == 2) {
            touch8k(Ax, 4u << 20, (unsigned)wa * 32768u + lw * 8192u, lane);
            touch8k(Wqkv, 6u << 20, (unsigned)wa * 49152u + lw * 8192u, lane);
            if (lw < 2) touch8k(Wqkv, 6u << 20, (unsigned)wa * 49152u + (4 + lw) * 8192u, lane);
            else touch8k(Wo, 2u << 20, (unsigned)wa * 16384u + (lw - 2) * 8192u, lane);
        }
        if (TOUCH0 == 3 || TOUCH0 == 5) touch8k(Ax, 4u << 20, (unsigned)wa * 32768u + lw * 8192u, lane);
        if (!(EARLY & 6)) wait_vm<22 + NT0>();
        if (!(EARLY & 6)) wg_barrier();
#pragma unroll 1
        for (int k = 0; k < 16; ++k) {

            if (k == 0) wait_vm<8 + NT0>(); else if (NPF && k < 17 - NPF) wait_vm<9>(); else if (k < 14) wait_vm<8>(); else wait_vm<NTA>();

            wg_barrier();

            if (k + 2 < 16) issue_n(k + 2);
            if (k + 3 < 16) issue_m(k + 3);
            if (NPF && k + NPF < 16) prefetch_n(k + NPF);
            if (TOUCHC == 5 && k >= TCK && k < TCK + 8 && (lw >> 1) == (k & 1))
                touch8k((lw & 1) ? Ci : Cr, 8u << 20, (unsigned)wa * 65536u + (unsigned)(k - TCK) * 8192u, lane);
            if (TOUCHC == 7 && k >= TCK && k < TCK + 8 && lw == (k & 3))
                touch8k(Ci, 8u << 20, (unsigned)wa * 65536u + (unsigned)(k - TCK) * 8192u, lane);
            if (NTA && k == 13) {
#pragma unroll
                for (int j = 0; j < 2; ++j) {
                    touch8k(Cr, 8u << 20, (unsigned)wa * 65536u + (lw * 2 + j) * 8192u, lane);
                    touch8k(Ci, 8u << 20, (unsigned)wa * 65536u + (lw * 2 + j) * 8192u, lane);
                }
                if (TOUCHW) touch4k(Wo, 2u << 20, (unsigned)wa * 16384u + lw * 4096u, lane);
            }

        }
    }
    wg_barrier();
    const u64 tag = lds_tag();
    if (!KVDIR && ib < 7) {
        char* img = ws + WS_KV + (size_t)(ib * 16 + p) * 65536;
        const unsigned off = (unsigned)lw * 16384u + (unsigned)lane * 16u;
        u32x4 tmp[16];
#pragma unroll
        for (int i = 0; i < 16; ++i) tmp[i] = *(LAS u32x4*)(size_t)(off + i * 1024u);
#pragma unroll
        for (int i = 0; i < 16; ++i) store16_dev(img, off + i * 1024u, tmp[i]);
        wait_vm<0>();
        if (lane == 0) {
            const unsigned old = __hip_atomic_fetch_add((LAS unsigned*)(size_t)LDS_CNT, 1u, __ATOMIC_RELAXED, __HIP_MEMORY_SCOPE_WORKGROUP);
            if (old == 3u) flag_store(ws, CTL_KV + ib * 16 + p, tag);
        }
    }
    if (ib > 0) {
        if (lw == 0) {
            for (int spin = 0; spin < SPIN_LIMIT && flag_load(ws, CTL_KV + (ib - 1) * 16 + p) != tag; ++spin) __builtin_amdgcn_s_sleep(2);
            lds_set(LDS_RDY_KV, 1u);
        } else {
            lds_wait_word(LDS_RDY_KV);
        }
        const i32x4 srdk = make_srd(ws + WS_KV + (size_t)((ib - 1) * 16 + p) * 65536, 65536);
        const unsigned off = (unsigned)lw * 16384u;
#pragma unroll
        for (int i = 0; i < 16; ++i) dma16_dev((unsigned)lane * 16u, srdk, off + i * 1024u, LDS_PREV + off + i * 1024u);
        wait_vm<0>();
    }
    wg_barrier();
    wg_barrier();
    if (lw == 0 && lane == 0) flag_store(ws, CTL_FO + ib * 16 + p, tag);
    {
        const int r = lane >> 4, P = lane & 15;
        const unsigned c = (unsigned)((P >> 1) ^ (r | (((lw >> 1) & 1) << 2)));
        const unsigned vn = (unsigned)(16 * (lw & 1) + 4 * (lw >> 1) + r) * 2048u + c * 32u + (unsigned)(P & 1) * 16u;
        const i32x4 srdn = make_srd(Wo + (size_t)(p >> 1) * 256, 1024 * 2048 - (p >> 1) * 256);
        const i32x4 srdm = make_srd(ws + WS_O + (size_t)ib * 16 * 32768 + (size_t)(p & 1) * 16384, 16 * 32768 - (p & 1) * 16384);
        const unsigned ldsw = (unsigned)lw * 1024u, vm = (unsigned)lane * 16u + ldsw;
        auto issue_n = [&](int k) {
            const unsigned lds = (unsigned)(k % ORING) * 32768u + 16384u + ldsw;
            const unsigned kk = OFIRST ? (unsigned)(k + p) & 15u : (unsigned)k;
#pragma unroll
            for (int i = 0; i < 4; ++i)
                dma16(vn, srdn, (kk * 64u + 32u * (i >> 1) + 8u * (i & 1)) * 2048u, lds + i * 4096u);
        };
        auto issue_m = [&](int k) {
            const unsigned lds = (unsigned)(k % ORING) * 32768u + ldsw;
#pragma unroll
            for (int i = 0; i < 4; ++i)
                dma16_dev(vm, srdm, (OFIRST ? (unsigned)(k + p) & 15u : (unsigned)k) * 32768u + i * 4096u, lds + i * 4096u);
        };
        if (!OLD8) {
#pragma unroll
            for (int k = 0; k < 4; ++k) issue_n(k);
        }
        if (lw == 0) {
            for (int spin = 0; spin < SPIN_LIMIT; ++spin) {
                if (__all(flag_load(ws, CTL_FO + ib * 16 + (lane & 15)) == tag)) break;
                __builtin_amdgcn_s_sleep(2);
            }
            lds_set(LDS_RDY_O, 1u);
        } else {
            lds_wait_word(LDS_RDY_O);
        }
#pragma unroll
        for (int k = 0; k < 4; ++k) issue_m(k);
        wait_vm<12>();
        wg_barrier();
#pragma unroll 1
        for (int k = 0; k < 16; ++k) {
            if (k == 0) wait_vm<8>(); else if (k == 1) wait_vm<12>(); else if (k < 13) wait_vm<16>(); else if (k == 13) wait_vm<8>(); else wait_vm<0>();
            wg_barrier();
            if (k + 4 < 16) { issue_n(k + 4); issue_m(k + 4); }
        }
    }
}

__global__ __launch_bounds__(768) void fused_kernel(const char* __restrict__ Ax, const char* __restrict__ Wqkv, const char* __restrict__ Wo,
                                                    const char* __restrict__ Bx, const char* __restrict__ Cr, const char* __restrict__ Ci,
                                                    const char* __restrict__ Dr, const char* __restrict__ Di, char* out, char* ws) {
    const int tid = threadIdx.x, lane = tid & 63, wid = uni(tid >> 6);
    const int w = blockIdx.x, x = w & 7, idx = w >> 3;
    if (CODE_BYTES > 0 && wid == 3) {
        u64 pc;
        asm volatile("s_getpc_b64 %0" : "=s"(pc));
        const char* code = (const char*)(pc & ~(u64)127);
#pragma unroll
        for (int i = 0; i < (CODE_BYTES + 8191) / 8192; ++i) touch8k(code, CODE_BYTES, i * 8192u, lane);
    }
    if (TOUCH0 == 1) {
        const unsigned wr = (unsigned)(((idx & 15) << 3) | x);
        if (idx < 16) {
            if (wid >= 4 && wid < 8) touch8k(Bx, 4u << 20, wr * 32768u + (wid - 4) * 8192u, lane);
            else if (wid < 4) touch8k(wid < 2 ? Dr : Di, 2u << 20, wr * 16384u + (wid & 1) * 8192u, lane);
        } else if (wid >= 4 && wid < 8) {
            const unsigned j = wid - 4;
            touch8k(Ax, 4u << 20, wr * 32768u + j * 8192u, lane);
            touch8k(Wqkv, 6u << 20, wr * 49152u + j * 8192u, lane);
            if (j < 2) touch8k(Wqkv, 6u << 20, wr * 49152u + (4 + j) * 8192u, lane);
            else touch8k(Wo, 2u << 20, wr * 16384u + (j - 2) * 8192u, lane);
        }
    }
    if (TOUCHC == 1 && idx >= 16 && wid < 8) {
        const unsigned wr = (unsigned)(((idx & 15) << 3) | x);
        touch8k(Cr, 8u << 20, wr * 65536u + wid * 8192u, lane);
        touch8k(Ci, 8u << 20, wr * 65536u + wid * 8192u, lane);
    }
    if (idx < 16) {
        const int eb = 4 * (x & 1) + (idx & 3), sb = 4 * (x >> 1) + (idx >> 2);
        const int h = (x >> 1) & 1, tb = 4 * (x >> 2) + (idx >> 2), wb = (idx << 3) | x;
        if (wid < 8) spectral_consumer(wid, lane, ws, out, w, wb, eb, sb, tb, h);
        else spectral_loader(wid - 8, lane, Bx, Cr, Ci, Dr, Di, ws, w, wb, eb, sb, tb, h);
    } else {
        const int a = idx - 16, ib = 4 * (x & 1) + (a & 3), p = 4 * (x >> 1) + (a >> 2);
        if (wid < 8) attn_consumer(wid, lane, ws, out, Wo, Cr, Ci, w, (a << 3) | x, ib, p);
        else attn_loader(wid - 8, lane, Ax, Wqkv, Wo, Cr, Ci, ws, (a << 3) | x, ib, p);
    }
}

extern "C" void kernel_launch(void* const* d_in, const int* in_sizes, int n_in, void* d_out, int out_size, void* d_ws, size_t ws_size,
                              hipStream_t stream) {
    (void)in_sizes; (void)n_in; (void)out_size; (void)ws_size;
    (void)hipFuncSetAttribute((const void*)fused_kernel, hipFuncAttributeMaxDynamicSharedMemorySize, LDS_BYTES);
    hipLaunchKernelGGL(fused_kernel, dim3(256), dim3(768), LDS_BYTES, stream, (const char*)d_in[0], (const char*)d_in[1],
                       (const char*)d_in[2], (const char*)d_in[3], (const char*)d_in[4], (const char*)d_in[5], (const char*)d_in[6],
                       (const char*)d_in[7], (char*)d_out, (char*)d_ws);
}
```
